```python
import torch

def _softmax(x, axis=-1):
    return torch.softmax(x, dim=axis)
def _gelu(x):
    return torch.nn.functional.gelu(x, approximate="tanh")
def _silu(x):
    return torch.nn.functional.silu(x)
def _sigmoid(x):
    return torch.sigmoid(x)
def _layernorm(x, axis=-1, eps=1e-5):
    m = x.mean(dim=axis, keepdim=True)
    v = x.var(dim=axis, keepdim=True, unbiased=False)
    return (x - m) / torch.sqrt(v + eps)
def _scatter_add(tgt, idx, src, axis=0):
    return tgt.clone().index_add_(axis, idx, src)
def _segment_sum(src, seg, n):
    out = torch.zeros((n,) + src.shape[1:], dtype=src.dtype)
    return out.index_add_(0, seg, src)
def _pad(x, pw):
    flat = tuple(v for pair in reversed(pw) for v in pair)
    return torch.nn.functional.pad(x, flat)

kernel_name = 'parallel_sum__ssm_conv_s4d__dense_activation_gelu__prompt'

def setup_inputs():

    _w = (lambda a: (lambda t: t.to(torch.bfloat16) if t.is_floating_point() else t)(torch.as_tensor(a)))
    def _A_setup():
        Bsz, L, D, N = 1, 2048, 512, 128
        return {
            "u": _w((lambda *s: torch.randn(*s, dtype=torch.bfloat16) * 0.05)(Bsz, L, D)),
            "log_A_real": _w((lambda *s: torch.randn(*s, dtype=torch.bfloat16) * 12.8)(D, N) * 0.5),
            "A_imag": _w((lambda *s: torch.randn(*s, dtype=torch.bfloat16) * 0.05)(D, N) * 3.14),
            "C_real": _w((lambda *s: torch.randn(*s, dtype=torch.bfloat16) * 0.1)(D, N) / N ** 0.5),
            "C_imag": _w((lambda *s: torch.randn(*s, dtype=torch.bfloat16) * 0.2)(D, N) / N ** 0.5),
            "log_dt": _w((lambda *s: torch.randn(*s, dtype=torch.bfloat16) * 0.05)(D) * 0.5 - 4.0),
            "D_skip": _w((lambda *s: torch.randn(*s, dtype=torch.bfloat16) * 0.05)(D)),
        }
    _A_in = _A_setup()

    def _B_setup():
        B, S, D, F = 1, 2048, 512, 1792
        return {
            "x": _w((lambda *s: torch.randn(*s, dtype=torch.bfloat16) * 0.05)(B, S, D)),
            "w1": _w((lambda *s: torch.randn(*s, dtype=torch.bfloat16) * 0.8)(D, F) * (D ** -0.5)),
            "b1": _w((lambda *s: torch.randn(*s, dtype=torch.bfloat16) * 12.8)(F) * 0.01),
            "w2": _w((lambda *s: torch.randn(*s, dtype=torch.bfloat16) * 0.8)(F, D) * (F ** -0.5)),
            "b2": _w((lambda *s: torch.randn(*s, dtype=torch.bfloat16) * 12.8)(D) * 0.01),
        }
    _B_in = _B_setup()

    return {
        "A_u": _A_in["u"],
        "A_log_A_real": _A_in["log_A_real"],
        "A_A_imag": _A_in["A_imag"],
        "A_C_real": _A_in["C_real"],
        "A_C_imag": _A_in["C_imag"],
        "A_log_dt": _A_in["log_dt"],
        "A_D_skip": _A_in["D_skip"],
        "B_x": _B_in["x"],
        "B_w1": _B_in["w1"],
        "B_b1": _B_in["b1"],
        "B_w2": _B_in["w2"],
        "B_b2": _B_in["b2"],
    }

def reference(A_u, A_log_A_real, A_A_imag, A_C_real, A_C_imag, A_log_dt, A_D_skip, B_x, B_w1, B_b1, B_w2, B_b2):
    def _A_ref(u, log_A_real, A_imag, C_real, C_imag, log_dt, D_skip):
        L = u.shape[1]
        dt = torch.exp(log_dt)
        A = -torch.exp(log_A_real) + 1j * A_imag
        C = (C_real + 1j * C_imag) * dt[:, None]
        pos = (lambda x, dt: x.to(dt))(torch.arange(L), log_dt.dtype)
        P = torch.exp((A * dt[:, None])[:, :, None] * pos[None, None, :])
        K = 2.0 * torch.einsum("dn,dnl->dl", C, P).real
        L2 = 2 * L
        u_t = (lambda x, *d: x.permute(*d))(u, 0, 2, 1)
        Kf = (lambda x, *a, **k: torch.fft.rfft(x.float(), *a, **k))(K, L2)
        Uf = (lambda x, *a, **k: torch.fft.rfft(x.float(), *a, **k))(u_t, L2)
        y = (lambda x, *a, **k: torch.fft.irfft(x, *a, **k).to(torch.bfloat16))(Uf * Kf[None, :, :], L2)[:, :, :L]
        y = (lambda x, *d: x.permute(*d))(y, 0, 2, 1)
        return y + u * D_skip[None, None, :]

    def _B_ref(x, w1, b1, w2, b2):
        h = torch.einsum("bsd,df->bsf", x, w1) + b1
        h = _gelu(h)
        return torch.einsum("bsf,fd->bsd", h, w2) + b2

    x = A_u
    xn = _layernorm(x, axis=-1)
    return x + _A_ref(xn, A_log_A_real, A_A_imag, A_C_real, A_C_imag, A_log_dt, A_D_skip) + _B_ref(xn, B_w1, B_b1, B_w2, B_b2)
```

```cpp
#include <hip/hip_runtime.h>
#include <hip/hip_bf16.h>
#include <stdint.h>

typedef unsigned short u16;
__device__ __forceinline__ float bf2f(u16 v){ return __uint_as_float(((unsigned)v)<<16); }
__device__ __forceinline__ u16 f2bf(float f){ unsigned u=__float_as_uint(f); unsigned r=(u+0x7FFFu+((u>>16)&1u))>>16; return (u16)r; }
__device__ __forceinline__ float rbf(float f){ return bf2f(f2bf(f)); }

constexpr int L=2048, D=512, N=128, F=1792;

__global__ void k_ln(const u16* x, u16* xn, u16* xnT){
  int t=blockIdx.x; int tid=threadIdx.x;
  __shared__ float red[256];
  float a=bf2f(x[t*D+tid]), b=bf2f(x[t*D+256+tid]);
  red[tid]=a+b; __syncthreads();
  for(int s=128;s>0;s>>=1){ if(tid<s) red[tid]+=red[tid+s]; __syncthreads(); }
  float mean=red[0]/D; __syncthreads();
  float da=a-mean, db=b-mean;
  red[tid]=da*da+db*db; __syncthreads();
  for(int s=128;s>0;s>>=1){ if(tid<s) red[tid]+=red[tid+s]; __syncthreads(); }
  float var=red[0]/D;
  float m=rbf(mean), v=rbf(var);
  float s=rbf(sqrtf(rbf(v+1e-5f)));
  u16 ra=f2bf(rbf(a-m)/s), rb=f2bf(rbf(b-m)/s);
  xn[t*D+tid]=ra; xn[t*D+256+tid]=rb;
  xnT[tid*L+t]=ra; xnT[(256+tid)*L+t]=rb;
}

__global__ void k_kgen(const u16* logA,const u16* Aim,const u16* Cr,const u16* Ci,const u16* logdt,float* K){
  int d=blockIdx.y; int l=blockIdx.x*256+threadIdx.x;
  float dt=rbf(expf(bf2f(logdt[d])));
  float p=rbf((float)l);
  float acc=0.f;
  for(int n=0;n<N;n++){
    float a=-rbf(expf(bf2f(logA[d*N+n])))*dt;
    float b=bf2f(Aim[d*N+n])*dt;
    float cr=bf2f(Cr[d*N+n])*dt, ci=bf2f(Ci[d*N+n])*dt;
    float e=expf(a*p); float sn,cs; sincosf(b*p,&sn,&cs);
    acc+=cr*(e*cs)-ci*(e*sn);
  }
  K[d*L+l]=2.f*acc;
}

__global__ void k_conv(const float* K,const u16* xnT,u16* yT){
  int d=blockIdx.y; int t=blockIdx.x*256+threadIdx.x;
  float acc=0.f;
  for(int l=0;l<=t;l++) acc+=K[d*L+l]*bf2f(xnT[d*L+t-l]);
  yT[d*L+t]=f2bf(acc);
}

__global__ void k_gemm1(const u16* xn,const u16* w1,const u16* b1,u16* h){
  int t=blockIdx.y; int f=blockIdx.x*256+threadIdx.x;
  float acc=0.f;
  for(int k=0;k<D;k++) acc+=bf2f(xn[t*D+k])*bf2f(w1[k*F+f]);
  float v=rbf(rbf(acc)+bf2f(b1[f]));
  float g=0.5f*v*(1.f+tanhf(0.7978845608028654f*(v+0.044715f*v*v*v)));
  h[t*F+f]=f2bf(g);
}

__global__ void k_gemm2(const u16* h,const u16* w2,const u16* b2,const u16* x,const u16* xn,const u16* yT,const u16* Dsk,u16* out){
  int t=blockIdx.y; int d=blockIdx.x*256+threadIdx.x;
  float acc=0.f;
  for(int k=0;k<F;k++) acc+=bf2f(h[t*F+k])*bf2f(w2[k*D+d]);
  float B=rbf(rbf(acc)+bf2f(b2[d]));
  float A=rbf(bf2f(yT[d*L+t])+rbf(bf2f(xn[t*D+d])*bf2f(Dsk[d])));
  out[t*D+d]=f2bf(rbf(bf2f(x[t*D+d])+A)+B);
}

extern "C" void kernel_launch(void* const* d_in, const int* in_sizes, int n_in,
                              void* d_out, int out_size, void* d_ws, size_t ws_size,
                              hipStream_t stream) {
  const u16* x=(const u16*)d_in[0];
  const u16* logA=(const u16*)d_in[1]; const u16* Aim=(const u16*)d_in[2];
  const u16* Cr=(const u16*)d_in[3]; const u16* Ci=(const u16*)d_in[4];
  const u16* logdt=(const u16*)d_in[5]; const u16* Dsk=(const u16*)d_in[6];
  const u16* w1=(const u16*)d_in[8]; const u16* b1=(const u16*)d_in[9];
  const u16* w2=(const u16*)d_in[10]; const u16* b2=(const u16*)d_in[11];
  char* ws=(char*)d_ws;
  u16* xn=(u16*)ws; ws+=L*D*2;
  u16* xnT=(u16*)ws; ws+=L*D*2;
  u16* yT=(u16*)ws; ws+=L*D*2;
  float* K=(float*)ws; ws+=L*D*4;
  u16* h=(u16*)ws; ws+=(size_t)L*F*2;
  k_ln<<<L,256,0,stream>>>(x,xn,xnT);
  k_kgen<<<dim3(L/256,D),256,0,stream>>>(logA,Aim,Cr,Ci,logdt,K);
  k_conv<<<dim3(L/256,D),256,0,stream>>>(K,xnT,yT);
  k_gemm1<<<dim3(F/256,L),256,0,stream>>>(xn,w1,b1,h);
  k_gemm2<<<dim3(D/256,L),256,0,stream>>>(h,w2,b2,x,xn,yT,Dsk,(u16*)d_out);
}
```

```cpp
#include <hip/hip_runtime.h>
#include <hip/hip_bf16.h>
#include <stdint.h>

typedef unsigned short u16;
typedef unsigned int u32;
typedef unsigned long long u64;
typedef __attribute__((ext_vector_type(8))) short bf16x8;
typedef __attribute__((ext_vector_type(4))) short s16x4;
typedef __attribute__((ext_vector_type(4))) float f32x4;
typedef __attribute__((ext_vector_type(4))) unsigned int u32x4;
typedef __attribute__((ext_vector_type(2))) unsigned int u32x2;
typedef __attribute__((ext_vector_type(2))) __bf16 bf2_t;
typedef __attribute__((ext_vector_type(2))) float f2_t;

#define DEV __device__ __forceinline__

constexpr int L = 2048, D = 512, NS = 128, F = 1792;
constexpr int KRP = 2064;

DEV float bf2f(u16 v) { return __uint_as_float(((u32)v) << 16); }
DEV u32 pk_bf16(float lo, float hi) {
  f2_t v = {lo, hi};
  bf2_t r = __builtin_convertvector(v, bf2_t);
  return __builtin_bit_cast(u32, r);
}
DEV float lo_f(u32 p) { return __uint_as_float(p << 16); }
DEV float hi_f(u32 p) { return __uint_as_float(p & 0xffff0000u); }
DEV float rbf(float f) { return lo_f(pk_bf16(f, f)); }

#define STAMP(kid, i)
#define WAIT_V(n) asm volatile("s_waitcnt vmcnt(" #n ")" ::: "memory")
#define CFENCE() asm volatile("" ::: "memory")

DEV f32x4 mfma16(bf16x8 a, bf16x8 b, f32x4 c) {
  return __builtin_amdgcn_mfma_f32_16x16x32_bf16(a, b, c, 0, 0, 0);
}

typedef __attribute__((address_space(3))) s16x4 lds_s16x4;
DEV s16x4 tr_read(const char* p) {
  return __builtin_amdgcn_ds_read_tr16_b64_v4i16((lds_s16x4*)p);
}

constexpr int K1_RING = 131072;
constexpr int K1_KP = K1_RING;
constexpr int K1_CS = K1_KP + 24576;
constexpr int K1_SMEM = K1_CS + 512 + 1024;

#define GLDS16(src, dst)                                                                     \
  __builtin_amdgcn_global_load_lds((const __attribute__((address_space(1))) void*)(src),     \
                                   (__attribute__((address_space(3))) void*)(dst), 16, 0, 0)
#define LGKM0() __builtin_amdgcn_s_waitcnt(0xC07F)
#define RAW_BAR() do { LGKM0(); CFENCE(); __builtin_amdgcn_s_barrier(); CFENCE(); } while (0)

DEV float gelu_tanh(float v) {
  const float c1 = -2.0f * 0.7978845608028654f * 1.4426950408889634f;
  const float c2 = c1 * 0.044715f;
  float u = v * v;
  float z = v * __builtin_fmaf(u, c2, c1);
  float e = __builtin_amdgcn_exp2f(z);
  return v * __builtin_amdgcn_rcpf(1.0f + e);
}

DEV int kpos_row(int rt, int row) {
  if (rt == 0) return row;
  if (rt == 1) return (row < 8) ? (256 + 2 * row) : (512 + 4 * (row & 3) + 256 * ((row >> 2) & 1));
  return (row < 8) ? (1024 + 8 * (row & 1) + 256 * (row >> 1)) : ((row == 8) ? 2048 : 0);
}
struct KIdxTab { u16 v[KRP]; };
constexpr int c_rne_bf16(int l) {
  if (l < 256) return l;
  int sh = (l < 512) ? 1 : (l < 1024) ? 2 : 3;
  return ((l + (1 << (sh - 1)) - 1 + ((l >> sh) & 1)) >> sh) << sh;
}
constexpr int c_kpos_index(int p) {
  int rt = 0, row = 0, lo = 0;
  if (p < 256) { rt = 0; row = p & 15; lo = p >> 4; }
  else if (p < 512) { int q = p - 256; rt = 1; row = (q & 15) >> 1; lo = q >> 4; }
  else if (p < 1024) { int q = p - 512; rt = 1; row = 8 + ((q & 15) >> 2) + 4 * (q >> 8); lo = (q & 255) >> 4; }
  else if (p < 2048) { int q = p - 1024; rt = 2; row = ((q & 15) >> 3) + 2 * (q >> 8); lo = (q & 255) >> 4; }
  else { rt = 2; row = 8; lo = 0; }
  return (rt * 64 + lo + 16 * (row >> 2)) * 4 + (row & 3);
}
constexpr KIdxTab make_kidx() {
  KIdxTab t{};
  for (int y = 0; y < KRP; ++y) {
    int l = 2047 - y;
    t.v[y] = (l < 0) ? (u16)0xFFFF : (u16)c_kpos_index(c_rne_bf16(l));
  }
  return t;
}
__device__ __attribute__((aligned(16))) const KIdxTab g_kidx = make_kidx();

DEV int kpos_index(int p) {
  int rt, row, lo;
  if (p < 256) { rt = 0; row = p & 15; lo = p >> 4; }
  else if (p < 512) { int q = p - 256; rt = 1; row = (q & 15) >> 1; lo = q >> 4; }
  else if (p < 1024) { int q = p - 512; rt = 1; row = 8 + ((q & 15) >> 2) + 4 * (q >> 8); lo = (q & 255) >> 4; }
  else if (p < 2048) { int q = p - 1024; rt = 2; row = ((q & 15) >> 3) + 2 * (q >> 8); lo = (q & 255) >> 4; }
  else { rt = 2; row = 8; lo = 0; }
  return (rt * 64 + lo + 16 * (row >> 2)) * 4 + (row & 3);
}

typedef __attribute__((ext_vector_type(8))) _Float16 f16x8;
typedef __attribute__((ext_vector_type(2))) _Float16 h2_t;
DEV u32 pk_f16(float lo, float hi) {
  f2_t v = {lo, hi};
  h2_t r = __builtin_convertvector(v, h2_t);
  return __builtin_bit_cast(u32, r);
}
DEV f32x4 mfma16h(u32x4 a, u32x4 b, f32x4 c) {
  return __builtin_amdgcn_mfma_f32_16x16x32_f16(__builtin_bit_cast(f16x8, a), __builtin_bit_cast(f16x8, b), c, 0, 0, 0);
}
DEV void split2(float v0, float v1, u32& hi, u32& lo) {
  hi = pk_bf16(v0, v1);
  lo = pk_bf16(v0 - lo_f(hi), v1 - hi_f(hi));
}

DEV bf16x8 tr_frag(const char* p, int d2) {
  s16x4 a0 = tr_read(p);
  s16x4 a1 = tr_read(p + d2);
  return (bf16x8){a0[0], a0[1], a0[2], a0[3], a1[0], a1[1], a1[2], a1[3]};
}

template <int NH>
DEV void gemm1_loop(char* smem, int bj, int xadr0, const int (&adr)[4], int adrc,
                    f32x4 (&acc)[2][4], f32x4& cs, f32x4 (&sm)[2], f32x4 (&sq)[2]) {
  const short one = 0x3f80;
  const bf16x8 ones = {one, one, one, one, one, one, one, one};
  constexpr int NT = NH ? 3 : 4;
  bf16x8 xf[2][2], a[2][NT], ac[2];
  auto rd = [&](int u, int slot) {
    const int s = u >> 1, k2 = u & 1;
    const char* xb = smem + (s & 3) * 32768;
    const char* wb = xb + 16384 + k2 * 8192;
    xf[slot][0] = *(const bf16x8*)(xb + (xadr0 ^ (64 * k2)));
    xf[slot][1] = *(const bf16x8*)(xb + (xadr0 ^ (64 * k2)) + 2048);
#pragma unroll
    for (int T = 0; T < NT; ++T) a[slot][T] = tr_frag(wb + adr[T], 1024);
    ac[slot] = tr_frag(wb + adrc, 1024);
  };
  CFENCE(); __builtin_amdgcn_s_barrier(); CFENCE();
  rd(0, 0);
#pragma unroll
  for (int u = 0; u < 16; ++u) {
    const int c = u & 1;
    if (u + 1 < 16) {
      if (u & 1) {
        RAW_BAR();
        STAMP(0, 4 + (u >> 1));
      }
      rd(u + 1, c ^ 1);
    }
#pragma unroll
    for (int T = 0; T < NT; ++T) {
      acc[0][T] = mfma16(a[c][T], xf[c][0], acc[0][T]);
      acc[1][T] = mfma16(a[c][T], xf[c][1], acc[1][T]);
    }
    cs = mfma16(ac[c], ones, cs);
    if (NH) {
      sm[0] = mfma16(ones, xf[c][0], sm[0]);
      sq[0] = mfma16(xf[c][0], xf[c][0], sq[0]);
      sm[1] = mfma16(ones, xf[c][1], sm[1]);
      sq[1] = mfma16(xf[c][1], xf[c][1], sq[1]);
    }
    if (u + 1 < 16) {
#pragma unroll
      for (int i = 0; i < 7; ++i) {
        __builtin_amdgcn_sched_group_barrier(0x8, 1, 0);
        __builtin_amdgcn_sched_group_barrier(0x100, 2, 0);
      }
    }
    __builtin_amdgcn_sched_barrier(0);
  }
}

template <int NH>
DEV void gemm1_epi(const float* cs_lds, const float* st_lds, const u16* __restrict__ b1,
                   u16* __restrict__ h, int t0, int n0, int wt, int r, int g, f32x4 (&acc)[2][4]) {
  constexpr int NT = NH ? 3 : 4;
  constexpr int TB = NH ? 4 : 0;
  float mean_b[2], inv_s[2];
#pragma unroll
  for (int tt = 0; tt < 2; ++tt) {
    f2_t st = *(const f2_t*)(st_lds + 2 * (32 * wt + 16 * tt + r));
    mean_b[tt] = st[0];
    inv_s[tt] = 1.0f / st[1];
  }
  u32x2 hp[2][NT];
#pragma unroll
  for (int T = 0; T < NT; ++T) {
    f32x4 cs = *(const f32x4*)(cs_lds + 16 * (TB + T) + 4 * g);
    u32x2 bb = *(const u32x2*)(b1 + n0 + 16 * (TB + T) + 4 * g);
    f32x4 bv = {lo_f(bb[0]), hi_f(bb[0]), lo_f(bb[1]), hi_f(bb[1])};
#pragma unroll
    for (int tt = 0; tt < 2; ++tt) {
      const float ms = mean_b[tt] * inv_s[tt];
      f32x4 v = acc[tt][T] * inv_s[tt] + (bv - cs * ms);
      const float c1 = -2.0f * 0.7978845608028654f * 1.4426950408889634f;
      const float c2 = c1 * 0.044715f;
      f32x4 z = v * (v * v * c2 + c1);
      f32x4 d;
#pragma unroll
      for (int i = 0; i < 4; ++i) d[i] = __builtin_amdgcn_exp2f(z[i]);
      d = d + 1.0f;
#pragma unroll
      for (int i = 0; i < 4; ++i) d[i] = __builtin_amdgcn_rcpf(d[i]);
      v = v * d;
      hp[tt][T][0] = pk_bf16(v[0], v[1]);
      hp[tt][T][1] = pk_bf16(v[2], v[3]);
    }
  }
#pragma unroll
  for (int tt = 0; tt < 2; ++tt) {
    u16* hrow = h + (size_t)(t0 + 32 * wt + 16 * tt + r) * F + n0 + 16 * TB;
#pragma unroll
    for (int T = 0; T + 1 < NT; T += 2) {
      auto s0 = __builtin_amdgcn_permlane16_swap(hp[tt][T][0], hp[tt][T + 1][0], false, false);
      auto s1 = __builtin_amdgcn_permlane16_swap(hp[tt][T][1], hp[tt][T + 1][1], false, false);
      u32x4 o = {s0[0], s1[0], s0[1], s1[1]};
      *(u32x4*)(hrow + 16 * (T + (g & 1)) + 8 * (g >> 1)) = o;
    }
    if (NH) *(u32x2*)(hrow + 32 + 4 * g) = hp[tt][2];
  }
}

template <int WHICH>
DEV void k1_loader(char* smem, const u16* __restrict__ x, const u16* __restrict__ w1, int t0, int n0, int l,
                   int bj, u16* __restrict__ xnT) {
  const u16* src[2];
  bool ok[2];
  if (WHICH == 0) {
#pragma unroll
    for (int v = 0; v < 2; ++v) {
      src[v] = x + (size_t)(t0 + (l >> 3)) * D + 8 * ((l & 7) ^ ((l >> 4) + 4 * v));
      ok[v] = true;
    }
  } else {
#pragma unroll
    for (int v = 0; v < 2; ++v) {
      const int c16 = ((l & 15) - 2 * ((l >> 4) + 4 * v)) & 15;
      ok[v] = c16 < 14;
      src[v] = w1 + (size_t)(l >> 4) * F + n0 + c16 * 8;
    }
  }
  auto stage = [&](int s) {
    char* dst = smem + (s & 3) * 32768 + WHICH * 16384;
#pragma unroll
    for (int i = 0; i < 16; ++i) {
      if (WHICH == 0) {
        GLDS16(src[i & 1] + (size_t)(8 * i) * D + 64 * s, dst + i * 1024);
      } else {
        const int v = (i >> 1) & 1;
        if (ok[v]) GLDS16(src[v] + (size_t)(64 * s + 4 * i) * F, dst + i * 1024);
      }
    }
  };
  stage(0); stage(1); stage(2); stage(3);
  CFENCE(); __builtin_amdgcn_s_barrier(); CFENCE();
  WAIT_V(48);
  __builtin_amdgcn_s_barrier(); CFENCE();
#pragma unroll
  for (int s = 1; s < 8; ++s) {
    if (s <= 5) WAIT_V(32);
    else if (s == 6) WAIT_V(16);
    else WAIT_V(0);
    __builtin_amdgcn_s_barrier(); CFENCE();
    if (s + 3 < 8) stage(s + 3);
  }
  u32x4 xs[4];
#pragma unroll
  for (int i = 0; i < 4; ++i)
    xs[i] = *(const u32x4*)(x + (size_t)(t0 + 64 * WHICH + 16 * i + (l >> 2)) * D + 32 * bj + 8 * (l & 3));
  CFENCE(); __builtin_amdgcn_s_barrier(); CFENCE();
  const float* st_lds = (const float*)(smem + K1_CS + 512);
  u16* tb = (u16*)(smem + K1_KP + WHICH * 8192);
#pragma unroll
  for (int i = 0; i < 4; ++i) {
    const int tl = 16 * i + (l >> 2);
    f2_t st = *(const f2_t*)(st_lds + 2 * (64 * WHICH + tl));
#pragma unroll
    for (int e = 0; e < 4; ++e) {
      float v0 = rbf(lo_f(xs[i][e]) - st[0]) / st[1];
      float v1 = rbf(hi_f(xs[i][e]) - st[0]) / st[1];
      u32 pk = pk_bf16(v0, v1);
      tb[(8 * (l & 3) + 2 * e) * 72 + tl] = (u16)(pk & 0xffffu);
      tb[(8 * (l & 3) + 2 * e + 1) * 72 + tl] = (u16)(pk >> 16);
    }
  }
  LGKM0();
  CFENCE();
#pragma unroll
  for (int i = 0; i < 4; ++i) {
    const int dd = 8 * i + (l >> 3), ck = l & 7;
    u32x4 o = *(const u32x4*)(tb + dd * 72 + 8 * ck);
    *(u32x4*)(xnT + (size_t)(32 * bj + dd) * L + t0 + 64 * WHICH + 8 * ck) = o;
  }
}

__global__ __launch_bounds__(640, 3) void k1(const u16* __restrict__ x, const u16* __restrict__ w1,
                                             const u16* __restrict__ b1,
                                             const u16* __restrict__ logA, const u16* __restrict__ Aim,
                                             const u16* __restrict__ Cr, const u16* __restrict__ Ci,
                                             const u16* __restrict__ logdt, u16* __restrict__ h,
                                             u16* __restrict__ xnT, u16* __restrict__ Kr,
                                             float* __restrict__ stats, u32* __restrict__ cnt) {
  __shared__ __attribute__((aligned(16))) char smem[K1_SMEM];
  const int b = blockIdx.x;
  const int xcd = b & 7, idx = b >> 3;
  const int bi = 2 * xcd + (idx >> 4), bj = idx & 15;
  const int t0 = bi * 128, n0 = bj * 112;
  const int tid = threadIdx.x, l = tid & 63, r = l & 15, g = l >> 4;
  const int w = __builtin_amdgcn_readfirstlane(tid >> 6);
  if (w == 8) { k1_loader<0>(smem, x, w1, t0, n0, l, bj, xnT); return; }
  if (w == 9) { k1_loader<1>(smem, x, w1, t0, n0, l, bj, xnT); return; }
  const int wt = w & 3, nh = w >> 2;
  STAMP(0, 0);
  if (b == 0 && tid < 8) cnt[32 * tid] = 0u;

  const u32x4 kix0 = *(const u32x4*)(g_kidx.v + 8 * (tid & 255));
  const u32x4 kix1 = *(const u32x4*)(g_kidx.v + 8 * (256 + (tid & 1)));
  const int cc = w >> 2, ch = 2 * b + cc, qn = w & 3;
  f32x4* Kp = (f32x4*)(smem + K1_KP);
  {
    const int base = ch * NS + 32 * qn + 8 * g;
    bf16x8 vla = *(const bf16x8*)(logA + base);
    bf16x8 vai = *(const bf16x8*)(Aim + base);
    bf16x8 vcr = *(const bf16x8*)(Cr + base);
    bf16x8 vci = *(const bf16x8*)(Ci + base);
    const u32 ldt2 = ((const u32*)logdt)[b];
    const float dt = rbf(expf(cc ? hi_f(ldt2) : lo_f(ldt2)));
    const float pF = (float)(16 * r);
    float a2[8], br[8], cr[8], ci[8];
#pragma unroll
    for (int j = 0; j < 8; ++j) {
      float a = -rbf(__expf(bf2f((u16)vla[j]))) * dt;
      a2[j] = a * 1.4426950408889634f;
      br[j] = (bf2f((u16)vai[j]) * dt) * 0.15915494309189535f;
      cr[j] = bf2f((u16)vcr[j]) * dt;
      ci[j] = bf2f((u16)vci[j]) * dt;
    }
    STAMP(0, 1);
    float mx = 0.f;
#pragma unroll
    for (int j = 0; j < 8; ++j) mx = fmaxf(mx, fabsf(cr[j]) + fabsf(ci[j]));
    mx = fmaxf(mx, __shfl_xor(mx, 16));
    mx = fmaxf(mx, __shfl_xor(mx, 32));
    u32 ex_ = (__float_as_uint(mx) >> 23) & 255u;
    ex_ = ex_ < 1u ? 1u : (ex_ > 253u ? 253u : ex_);
    const float S = __uint_as_float((254u - ex_) << 23);
    const float outS = 2.0f * __uint_as_float(ex_ << 23);
    u32x4 Fr, Fi;
#pragma unroll
    for (int j2 = 0; j2 < 4; ++j2) {
      float fr[2], fi[2];
#pragma unroll
      for (int e = 0; e < 2; ++e) {
        int j = 2 * j2 + e;
        float ex = __builtin_amdgcn_exp2f(a2[j] * pF) * S;
        float an = __builtin_amdgcn_fractf(br[j] * pF);
        float er = ex * __builtin_amdgcn_cosf(an);
        float ei = ex * __builtin_amdgcn_sinf(an);
        fr[e] = cr[j] * er - ci[j] * ei;
        fi[e] = -(cr[j] * ei + ci[j] * er);
      }
      Fr[j2] = pk_f16(fr[0], fr[1]);
      Fi[j2] = pk_f16(fi[0], fi[1]);
    }
#pragma unroll
    for (int rt = 0; rt < 3; ++rt) {
      const float pG = (float)kpos_row(rt, r);
      u32x4 Gr, Gi;
#pragma unroll
      for (int j2 = 0; j2 < 4; ++j2) {
        float gr[2], gi[2];
#pragma unroll
        for (int e = 0; e < 2; ++e) {
          int j = 2 * j2 + e;
          float ex = __builtin_amdgcn_exp2f(a2[j] * pG);
          float an = __builtin_amdgcn_fractf(br[j] * pG);
          gr[e] = ex * __builtin_amdgcn_cosf(an);
          gi[e] = ex * __builtin_amdgcn_sinf(an);
        }
        Gr[j2] = pk_f16(gr[0], gr[1]);
        Gi[j2] = pk_f16(gi[0], gi[1]);
      }
      f32x4 a = {0.f, 0.f, 0.f, 0.f};
      a = mfma16h(Gr, Fr, a);
      a = mfma16h(Gi, Fi, a);
      Kp[(w * 3 + rt) * 64 + l] = a * outS;
    }
  }
  STAMP(0, 2);
  RAW_BAR();
  {
    auto expand = [&](const u32x4& kix, int c2, int chunk) {
      const float* kt = (const float*)(smem + K1_KP) + c2 * 4 * 768;
      float v[8];
#pragma unroll
      for (int e = 0; e < 8; ++e) {
        u32 ix = (e & 1) ? (kix[e >> 1] >> 16) : (kix[e >> 1] & 0xffffu);
        bool z = ix == 0xffffu;
        ix = z ? 0u : ix;
        float kv = (kt[ix] + kt[768 + ix]) + (kt[1536 + ix] + kt[2304 + ix]);
        v[e] = z ? 0.f : kv;
      }
      u32x4 o = {pk_bf16(v[0], v[1]), pk_bf16(v[2], v[3]), pk_bf16(v[4], v[5]), pk_bf16(v[6], v[7])};
      *(u32x4*)(Kr + (size_t)(2 * b + c2) * KRP + 8 * chunk) = o;
    };
    expand(kix0, tid >> 8, tid & 255);
    if (tid < 4) expand(kix1, tid >> 1, 256 + (tid & 1));
  }
  STAMP(0, 3);

  const int q = r >> 2, p = r & 3;
  const int rowoff = (8 * g + q) * 256 + 8 * (p & 1);
  const int prot = (p >> 1) + 2 * q + 8 * (g & 1);
  int adr[4];
#pragma unroll
  for (int T = 0; T < 4; ++T) adr[T] = rowoff + (((2 * (4 * nh + T) + prot) & 15) << 4);
  const int Tc = nh ? (4 + (wt > 2 ? 2 : wt)) : wt;
  const int adrc = rowoff + (((2 * Tc + prot) & 15) << 4);
  const int xadr0 = (32 * wt + r) * 128 + ((g ^ ((r >> 1) & 7)) << 4);

  f32x4 acc[2][4];
#pragma unroll
  for (int tt = 0; tt < 2; ++tt)
#pragma unroll
    for (int T = 0; T < 4; ++T) acc[tt][T] = (f32x4){0.f, 0.f, 0.f, 0.f};
  f32x4 cs = {0.f, 0.f, 0.f, 0.f};
  f32x4 sm[2] = {{0.f, 0.f, 0.f, 0.f}, {0.f, 0.f, 0.f, 0.f}}, sq[2] = {{0.f, 0.f, 0.f, 0.f}, {0.f, 0.f, 0.f, 0.f}};
  float* cs_lds = (float*)(smem + K1_CS);
  float* st_lds = (float*)(smem + K1_CS + 512);
  if (nh == 0) {
    gemm1_loop<0>(smem, bj, xadr0, adr, adrc, acc, cs, sm, sq);
    if (r == 0) *(f32x4*)(cs_lds + 16 * Tc + 4 * g) = cs;
    RAW_BAR();
    STAMP(0, 12);
    gemm1_epi<0>(cs_lds, st_lds, b1, h, t0, n0, wt, r, g, acc);
    STAMP(0, 13);
  } else {
    gemm1_loop<1>(smem, bj, xadr0, adr, adrc, acc, cs, sm, sq);
    if (r == 0) *(f32x4*)(cs_lds + 16 * Tc + 4 * g) = cs;
#pragma unroll
    for (int tt = 0; tt < 2; ++tt) {
      float cand = (p == 0) ? sq[tt][0] : (p == 1) ? sq[tt][1] : (p == 2) ? sq[tt][2] : sq[tt][3];
      float sumsq = __shfl(cand, r + 16 * q);
      float mean = sm[tt][0] * (1.0f / D);
      float var = fmaxf(sumsq * (1.0f / D) - mean * mean, 0.f);
      float m = rbf(mean), v = rbf(var);
      float s = rbf(sqrtf(rbf(v + 1e-5f)));
      if (g == 0) {
        f2_t st = {m, s};
        *(f2_t*)(st_lds + 2 * (32 * wt + 16 * tt + r)) = st;
        if (bj == 0) *(f2_t*)(stats + 2 * (t0 + 32 * wt + 16 * tt + r)) = st;
      }
    }
    STAMP(0, 11);
    RAW_BAR();
    STAMP(0, 12);
    gemm1_epi<1>(cs_lds, st_lds, b1, h, t0, n0, wt, r, g, acc);
    STAMP(0, 13);
    STAMP(0, 14);
  }
}

constexpr int CPITCH = 4160;
constexpr int CH_BYTES = 8 * CPITCH + 4608;
constexpr int K2_SMEM = 81920;

DEV u32 funnel16(u32 hi, u32 lo) { return __builtin_amdgcn_alignbyte(hi, lo, 2); }

DEV void conv_role(char* smem, const u16* __restrict__ Kr, const u16* __restrict__ xnT,
                   u16* __restrict__ yT, u32* __restrict__ cnt) {
  const int c = blockIdx.x;
  const int tid = threadIdx.x, l = tid & 63, r = l & 15, g = l >> 4;
  const int w = __builtin_amdgcn_readfirstlane(tid >> 6);
  STAMP(1, 0);
  auto put = [&](const u32x4& ka, const u32x4& kb, char* base) {
    u32 d[8] = {ka[0], ka[1], ka[2], ka[3], kb[0], kb[1], kb[2], kb[3]};
#pragma unroll
    for (int s = 0; s < 8; ++s) {
      const int e = 7 - s;
      u32x4 o;
#pragma unroll
      for (int i = 0; i < 4; ++i)
        o[i] = (e & 1) ? funnel16(d[(e + 1) / 2 + i], d[(e - 1) / 2 + i]) : d[e / 2 + i];
      *(u32x4*)(base + s * CPITCH) = o;
    }
  };
  {
    const int c2 = tid >> 8, ck = tid & 255;
    const u16* kr = Kr + (size_t)(2 * c + c2) * KRP + 8 * ck;
    u32x4 ka = *(const u32x4*)(kr), kb = *(const u32x4*)(kr + 8);
    u32x4 uu = *(const u32x4*)(xnT + (size_t)(2 * c) * L + 8 * tid);
    u32x4 kc, kd;
    if (tid < 2) {
      const u16* kr2 = Kr + (size_t)(2 * c + tid) * KRP + 8 * 256;
      kc = *(const u32x4*)(kr2); kd = *(const u32x4*)(kr2 + 8);
    }
    put(ka, kb, smem + c2 * CH_BYTES + 16 * ck);
    *(u32x4*)(smem + c2 * CH_BYTES + 8 * CPITCH + 512 + 16 * ck) = uu;
    if (tid < 2) put(kc, kd, smem + tid * CH_BYTES + 16 * 256);
    if (ck < 32) *(u32x4*)(smem + c2 * CH_BYTES + 8 * CPITCH + 16 * ck) = (u32x4){0u, 0u, 0u, 0u};
  }
  __syncthreads();
  STAMP(1, 1);
  const int cc = w >> 2, ch = 2 * c + cc, wq = w & 3;
  f32x4 acc[8];
#pragma unroll
  for (int i = 0; i < 8; ++i) acc[i] = (f32x4){0.f, 0.f, 0.f, 0.f};
  {
    const char* base = smem + cc * CH_BYTES;
    const char* ab = base + (r & 7) * CPITCH + 2 * (2048 - 8 * g - 8 * (r >> 3)) - 64 * (2 * wq);
    const char* bb = base + 8 * CPITCH + 2 * (256 + 16 * r - 8 * g + 8) - 64 * (2 * wq);
#pragma unroll
    for (int cl = 0; cl < 2; ++cl) {
      bf16x8 A[8], B[8];
#pragma unroll
      for (int pq = 0; pq < 8; ++pq) {
        A[pq] = *(const bf16x8*)(ab - 64 * cl - 512 * pq);
        B[pq] = *(const bf16x8*)(bb - 64 * cl + 512 * pq);
      }
#pragma unroll
      for (int p = 0; p < 8; ++p)
#pragma unroll
        for (int i = p; i < 8; ++i) acc[i] = mfma16(A[p], B[i - p], acc[i]);
    }
  }
  __syncthreads();
  STAMP(1, 2);
  f32x4* xb = (f32x4*)(smem + cc * CH_BYTES);
#pragma unroll
  for (int i = 0; i < 8; ++i) xb[(wq * 8 + i) * 64 + l] = acc[i];
  __syncthreads();
  {
    u16* yrow = yT + (size_t)ch * L + 16 * r + 4 * g + 512 * wq;
#pragma unroll
    for (int i = 0; i < 2; ++i) {
      const f32x4* p = xb + (2 * wq + i) * 64 + l;
      f32x4 v = (p[0] + p[512]) + (p[1024] + p[1536]);
      u64 o = (u64)pk_bf16(v[0], v[1]) | ((u64)pk_bf16(v[2], v[3]) << 32);
      __hip_atomic_store((u64*)(yrow + 256 * i), o, __ATOMIC_RELAXED, __HIP_MEMORY_SCOPE_AGENT);
    }
  }
  WAIT_V(0);
  __syncthreads();
  STAMP(1, 3);
  if (tid == 0)
    __hip_atomic_fetch_add(cnt + 32 * (c >> 5), 1u, __ATOMIC_RELAXED, __HIP_MEMORY_SCOPE_AGENT);
}

constexpr int G2_NR = 5, G2_NST = 28;

template <int WHICH>
DEV void g2_loader(char* smem, const u16* __restrict__ h, const u16* __restrict__ w2, int t0, int d0, int l,
                   int hf) {
  const u16* src[2];
#pragma unroll
  for (int v = 0; v < 2; ++v) {
    if (WHICH == 0) src[v] = h + (size_t)(t0 + 32 * hf + (l >> 3)) * F + 8 * ((l & 7) ^ ((l >> 4) + 4 * v));
    else src[v] = w2 + (size_t)(32 * hf + (l >> 3)) * D + d0 + 8 * ((l & 7) ^ ((((l >> 4) & 1) | (v << 1)) << 1));
  }
  char* dst0 = smem + WHICH * 8192 + hf * 4096;
  auto stage = [&](int s) {
    char* dst = dst0 + (s % G2_NR) * 16384;
#pragma unroll
    for (int i = 0; i < 4; ++i) {
      if (WHICH == 0) GLDS16(src[i & 1] + (size_t)(8 * i) * F + 64 * s, dst + i * 1024);
      else GLDS16(src[i & 1] + (size_t)(64 * s + 8 * i) * D, dst + i * 1024);
    }
  };
#pragma unroll
  for (int s = 0; s < G2_NR; ++s) stage(s);
  WAIT_V(16);
  __builtin_amdgcn_s_barrier(); CFENCE();
#pragma unroll
  for (int s = 0; s <= G2_NST - 2; ++s) {
    if (s + 4 <= G2_NST - 1) WAIT_V(12);
    else if (s + 3 == G2_NST - 1) WAIT_V(8);
    else if (s + 2 == G2_NST - 1) WAIT_V(4);
    else WAIT_V(0);
    __builtin_amdgcn_s_barrier(); CFENCE();
    if (s + G2_NR < G2_NST) stage(s + G2_NR);
  }
}

DEV void gemm2_role(char* smem, const u16* __restrict__ h, const u16* __restrict__ w2,
                    const u16* __restrict__ b2, const u16* __restrict__ x,
                    const u16* __restrict__ Dsk, const float* __restrict__ stats,
                    const u16* yT, u32* cnt, u16* __restrict__ out) {
  const int b = blockIdx.x - 256;
  const int xcd = b & 7, idx = b >> 3;
  const int ti = 4 * xcd + (idx >> 3), dj = idx & 7;
  const int t0 = 64 * ti, d0 = 64 * dj;
  const int tid = threadIdx.x, l = tid & 63, r = l & 15, g = l >> 4;
  const int w = __builtin_amdgcn_readfirstlane(tid >> 6);
  if (w >= 6) { g2_loader<1>(smem, h, w2, t0, d0, l, w & 1); return; }
  if (w >= 4) { g2_loader<0>(smem, h, w2, t0, d0, l, w & 1); return; }
  const int dh = w >> 1, th = w & 1;
  STAMP(1, 0);
  constexpr int NR = G2_NR, NST = G2_NST;

  const int q = r >> 2, p = r & 3;
  const int fr = ((q >> 1) & 1) | ((g & 1) << 1);
  int aadr[2];
#pragma unroll
  for (int a = 0; a < 2; ++a)
    aadr[a] = 8192 + (8 * g + q) * 128 + (((2 * (2 * dh + a) + (p >> 1)) ^ (fr << 1)) << 4) + 8 * (p & 1);
  const int badr = (32 * th + r) * 128 + ((g ^ ((r >> 1) & 7)) << 4);

  u32x4 xv[2], dv, bv;
  f2_t st[2];
  u64 yv[4];

  f32x4 acc[2][2];
#pragma unroll
  for (int i = 0; i < 2; ++i)
#pragma unroll
    for (int j = 0; j < 2; ++j) acc[i][j] = (f32x4){0.f, 0.f, 0.f, 0.f};

  bf16x8 af[2][2][2], bf[2][2][2];
  auto rd = [&](int s, int slot) {
    const char* sb = smem + (s % NR) * 16384;
#pragma unroll
    for (int k2 = 0; k2 < 2; ++k2) {
      bf[slot][k2][0] = *(const bf16x8*)(sb + (badr ^ (64 * k2)));
      bf[slot][k2][1] = *(const bf16x8*)(sb + (badr ^ (64 * k2)) + 2048);
#pragma unroll
      for (int a = 0; a < 2; ++a) af[slot][k2][a] = tr_frag(sb + aadr[a] + k2 * 4096, 512);
    }
  };
  u16* ytile = (u16*)(smem + ((NST - 5) % NR) * 16384);
  u32 cv = 0;
  bool got = false;
  auto step = [&](int sm10, bool more) {
    const int c = sm10 & 1;
    if (more) {
      RAW_BAR();
      rd(sm10 + 1, c ^ 1);
    }
#pragma unroll
    for (int k2 = 0; k2 < 2; ++k2)
#pragma unroll
      for (int a = 0; a < 2; ++a) {
        acc[a][0] = mfma16(af[c][k2][a], bf[c][k2][0], acc[a][0]);
        acc[a][1] = mfma16(af[c][k2][a], bf[c][k2][1], acc[a][1]);
      }
    if (more) {
#pragma unroll
      for (int i = 0; i < 6; ++i) {
        __builtin_amdgcn_sched_group_barrier(0x8, 1, 0);
        __builtin_amdgcn_sched_group_barrier(0x100, 2, 0);
      }
    }
    __builtin_amdgcn_sched_barrier(0);
  };
  CFENCE(); __builtin_amdgcn_s_barrier(); CFENCE();
  rd(0, 0);
#pragma unroll 1
  for (int s0 = 0; s0 < 20; s0 += 10) {
#pragma unroll
    for (int j = 0; j < 10; ++j) step(j, true);
  }
#pragma unroll
  for (int s = 20; s < NST; ++s) {
    step(s % 10, s + 1 < NST);
    if (s == 20) {
      cv = __hip_atomic_load(cnt + 32 * dj, __ATOMIC_RELAXED, __HIP_MEMORY_SCOPE_AGENT);
      __builtin_amdgcn_sched_barrier(0);
    }
    if (s == 23) {
      got = __builtin_amdgcn_readfirstlane(cv) >= 32u;
      CFENCE();
      int tz = tid;
      asm volatile("" : "+v"(tz));
      if (got) {
#pragma unroll
        for (int i = 0; i < 4; ++i) {
          int id = tz + 256 * i;
          yv[i] = __hip_atomic_load((const u64*)(yT + (size_t)(d0 + (id >> 4)) * L + t0 + 4 * (id & 15)),
                                    __ATOMIC_RELAXED, __HIP_MEMORY_SCOPE_AGENT);
        }
      }
      const int r2 = tz & 15, g2 = (tz >> 4) & 3;
      const int dl2 = 32 * dh + 16 * (g2 & 1) + 8 * (g2 >> 1);
#pragma unroll
      for (int bb = 0; bb < 2; ++bb) {
        const int t = t0 + 32 * th + 16 * bb + r2;
        xv[bb] = *(const u32x4*)(x + (size_t)t * D + d0 + dl2);
        st[bb] = *(const f2_t*)(stats + 2 * t);
      }
      dv = *(const u32x4*)(Dsk + d0 + dl2);
      bv = *(const u32x4*)(b2 + d0 + dl2);
      __builtin_amdgcn_sched_barrier(0);
    }
  }
  STAMP(1, 1);
  {
    int tz = tid;
    asm volatile("" : "+v"(tz));
    if (!got) {
      u32 spins = 0;
      while (__hip_atomic_load(cnt + 32 * dj, __ATOMIC_RELAXED, __HIP_MEMORY_SCOPE_AGENT) < 32u) {
        __builtin_amdgcn_s_sleep(2);
        if (++spins > (1u << 24)) break;
      }
      CFENCE();
#pragma unroll
      for (int i = 0; i < 4; ++i) {
        int id = tz + 256 * i;
        yv[i] = __hip_atomic_load((const u64*)(yT + (size_t)(d0 + (id >> 4)) * L + t0 + 4 * (id & 15)),
                                  __ATOMIC_RELAXED, __HIP_MEMORY_SCOPE_AGENT);
      }
    }
#pragma unroll
    for (int i = 0; i < 4; ++i) {
      int id = tz + 256 * i;
      *(u64*)(ytile + (id >> 4) * 72 + 4 * (id & 15)) = yv[i];
    }
    RAW_BAR();
    STAMP(1, 2);
    const int r2 = tz & 15, g2 = (tz >> 4) & 3;
    const int dl2 = 32 * dh + 16 * (g2 & 1) + 8 * (g2 >> 1);
#pragma unroll
    for (int bb = 0; bb < 2; ++bb) {
      float v[8];
#pragma unroll
      for (int i = 0; i < 4; ++i) {
        auto sw = __builtin_amdgcn_permlane16_swap(__float_as_uint(acc[0][bb][i]),
                                                   __float_as_uint(acc[1][bb][i]), false, false);
        v[i] = __uint_as_float(sw[0]);
        v[4 + i] = __uint_as_float(sw[1]);
      }
      const int tl = 32 * th + 16 * bb + r2;
      const u16* yp = ytile + dl2 * 72 + tl;
      const float inv_s = 1.0f / st[bb][1];
      float o[8];
#pragma unroll
      for (int e = 0; e < 8; ++e) {
        float xs = (e & 1) ? hi_f(xv[bb][e >> 1]) : lo_f(xv[bb][e >> 1]);
        float ds = (e & 1) ? hi_f(dv[e >> 1]) : lo_f(dv[e >> 1]);
        float bs = (e & 1) ? hi_f(bv[e >> 1]) : lo_f(bv[e >> 1]);
        float y = bf2f(yp[e * 72]);
        float xn = (xs - st[bb][0]) * inv_s;
        o[e] = (xs + (y + xn * ds)) + (v[e] + bs);
      }
      u32x4 ov = {pk_bf16(o[0], o[1]), pk_bf16(o[2], o[3]), pk_bf16(o[4], o[5]), pk_bf16(o[6], o[7])};
      *(u32x4*)(out + (size_t)(t0 + tl) * D + d0 + dl2) = ov;
    }
  }
  STAMP(1, 3);
}

__global__ __launch_bounds__(512, 4) void k2(const u16* __restrict__ Kr, const u16* __restrict__ xnT,
                                             u16* yT, u32* cnt, const u16* __restrict__ h,
                                             const u16* __restrict__ w2, const u16* __restrict__ b2,
                                             const u16* __restrict__ x, const u16* __restrict__ Dsk,
                                             const float* __restrict__ stats, u16* __restrict__ out) {
  __shared__ __attribute__((aligned(16))) char smem[K2_SMEM];
  if (blockIdx.x < 256) conv_role(smem, Kr, xnT, yT, cnt);
  if (blockIdx.x >= 256) gemm2_role(smem, h, w2, b2, x, Dsk, stats, yT, cnt, out);
}

extern "C" void kernel_launch(void* const* d_in, const int* in_sizes, int n_in, void* d_out,
                              int out_size, void* d_ws, size_t ws_size, hipStream_t stream) {
  const u16* x = (const u16*)d_in[0];
  const u16* logA = (const u16*)d_in[1];
  const u16* Aim = (const u16*)d_in[2];
  const u16* Cr = (const u16*)d_in[3];
  const u16* Ci = (const u16*)d_in[4];
  const u16* logdt = (const u16*)d_in[5];
  const u16* Dsk = (const u16*)d_in[6];
  const u16* w1 = (const u16*)d_in[8];
  const u16* b1 = (const u16*)d_in[9];
  const u16* w2 = (const u16*)d_in[10];
  const u16* b2 = (const u16*)d_in[11];
  char* ws = (char*)d_ws;
  u32* cnt = (u32*)ws; ws += 4096;
  float* stats = (float*)ws; ws += L * 2 * 4;
  u16* h = (u16*)ws; ws += (size_t)L * F * 2;
  u16* xnT = (u16*)ws; ws += (size_t)D * L * 2;
  u16* yT = (u16*)ws; ws += (size_t)D * L * 2;
  u16* Kr = (u16*)ws; ws += (size_t)D * KRP * 2;
  k1<<<256, 640, 0, stream>>>(x, w1, b1, logA, Aim, Cr, Ci, logdt, h, xnT, Kr, stats, cnt);
  k2<<<512, 512, 0, stream>>>(Kr, xnT, yT, cnt, h, w2, b2, x, Dsk, stats, (u16*)d_out);
}
```

```cpp
#include <hip/hip_runtime.h>
#include <hip/hip_bf16.h>
#include <stdint.h>

typedef unsigned short u16;
typedef unsigned int u32;
typedef unsigned long long u64;
typedef __attribute__((ext_vector_type(8))) short bf16x8;
typedef __attribute__((ext_vector_type(4))) short s16x4;
typedef __attribute__((ext_vector_type(4))) float f32x4;
typedef __attribute__((ext_vector_type(4))) unsigned int u32x4;
typedef __attribute__((ext_vector_type(2))) unsigned int u32x2;
typedef __attribute__((ext_vector_type(2))) __bf16 bf2_t;
typedef __attribute__((ext_vector_type(2))) float f2_t;

#define DEV __device__ __forceinline__

constexpr int L = 2048, D = 512, NS = 128, F = 1792;
constexpr int KRP = 2064;

DEV float bf2f(u16 v) { return __uint_as_float(((u32)v) << 16); }
DEV u32 pk_bf16(float lo, float hi) {
  f2_t v = {lo, hi};
  bf2_t r = __builtin_convertvector(v, bf2_t);
  return __builtin_bit_cast(u32, r);
}
DEV float lo_f(u32 p) { return __uint_as_float(p << 16); }
DEV float hi_f(u32 p) { return __uint_as_float(p & 0xffff0000u); }
DEV float rbf(float f) { return lo_f(pk_bf16(f, f)); }

#define STAMP(kid, i)
#define WAIT_V(n) asm volatile("s_waitcnt vmcnt(" #n ")" ::: "memory")
#define CFENCE() asm volatile("" ::: "memory")

DEV f32x4 mfma16(bf16x8 a, bf16x8 b, f32x4 c) {
  return __builtin_amdgcn_mfma_f32_16x16x32_bf16(a, b, c, 0, 0, 0);
}

typedef __attribute__((address_space(3))) s16x4 lds_s16x4;
DEV s16x4 tr_read(const char* p) {
  return __builtin_amdgcn_ds_read_tr16_b64_v4i16((lds_s16x4*)p);
}

constexpr int K1_RING = 131072;
constexpr int K1_KP = K1_RING;
constexpr int K1_CS = K1_KP + 24576;
constexpr int K1_PF = K1_CS + 512 + 1024;
constexpr int K1_SMEM = K1_PF + 4096;

#define GLDS16(src, dst)                                                                     \
  __builtin_amdgcn_global_load_lds((const __attribute__((address_space(1))) void*)(src),     \
                                   (__attribute__((address_space(3))) void*)(dst), 16, 0, 0)
#define LGKM0() __builtin_amdgcn_s_waitcnt(0xC07F)
#define RAW_BAR() do { LGKM0(); CFENCE(); __builtin_amdgcn_s_barrier(); CFENCE(); } while (0)

#define HSTORE4(p, v) (*(p) = (v))
#define HSTORE2(p, v) (*(p) = (v))
DEV float gelu_tanh(float v) {
  const float c1 = -2.0f * 0.7978845608028654f * 1.4426950408889634f;
  const float c2 = c1 * 0.044715f;
  float u = v * v;
  float z = v * __builtin_fmaf(u, c2, c1);
  float e = __builtin_amdgcn_exp2f(z);
  return v * __builtin_amdgcn_rcpf(1.0f + e);
}

DEV int kpos_row(int rt, int row) {
  if (rt == 0) return row;
  if (rt == 1) return (row < 8) ? (256 + 2 * row) : (512 + 4 * (row & 3) + 256 * ((row >> 2) & 1));
  return (row < 8) ? (1024 + 8 * (row & 1) + 256 * (row >> 1)) : ((row == 8) ? 2048 : 0);
}
struct KIdxTab { u16 v[KRP]; };
constexpr int c_rne_bf16(int l) {
  if (l < 256) return l;
  int sh = (l < 512) ? 1 : (l < 1024) ? 2 : 3;
  return ((l + (1 << (sh - 1)) - 1 + ((l >> sh) & 1)) >> sh) << sh;
}
constexpr int c_kpos_index(int p) {
  int rt = 0, row = 0, lo = 0;
  if (p < 256) { rt = 0; row = p & 15; lo = p >> 4; }
  else if (p < 512) { int q = p - 256; rt = 1; row = (q & 15) >> 1; lo = q >> 4; }
  else if (p < 1024) { int q = p - 512; rt = 1; row = 8 + ((q & 15) >> 2) + 4 * (q >> 8); lo = (q & 255) >> 4; }
  else if (p < 2048) { int q = p - 1024; rt = 2; row = ((q & 15) >> 3) + 2 * (q >> 8); lo = (q & 255) >> 4; }
  else { rt = 2; row = 8; lo = 0; }
  return (rt * 64 + lo + 16 * (row >> 2)) * 4 + (row & 3);
}
constexpr KIdxTab make_kidx() {
  KIdxTab t{};
  for (int y = 0; y < KRP; ++y) {
    int l = 2047 - y;
    t.v[y] = (l < 0) ? (u16)0xFFFF : (u16)c_kpos_index(c_rne_bf16(l));
  }
  return t;
}
__device__ __attribute__((aligned(16))) const KIdxTab g_kidx = make_kidx();

DEV int kpos_index(int p) {
  int rt, row, lo;
  if (p < 256) { rt = 0; row = p & 15; lo = p >> 4; }
  else if (p < 512) { int q = p - 256; rt = 1; row = (q & 15) >> 1; lo = q >> 4; }
  else if (p < 1024) { int q = p - 512; rt = 1; row = 8 + ((q & 15) >> 2) + 4 * (q >> 8); lo = (q & 255) >> 4; }
  else if (p < 2048) { int q = p - 1024; rt = 2; row = ((q & 15) >> 3) + 2 * (q >> 8); lo = (q & 255) >> 4; }
  else { rt = 2; row = 8; lo = 0; }
  return (rt * 64 + lo + 16 * (row >> 2)) * 4 + (row & 3);
}

typedef __attribute__((ext_vector_type(8))) _Float16 f16x8;
typedef __attribute__((ext_vector_type(2))) _Float16 h2_t;
DEV u32 pk_f16(float lo, float hi) {
  f2_t v = {lo, hi};
  h2_t r = __builtin_convertvector(v, h2_t);
  return __builtin_bit_cast(u32, r);
}
DEV f32x4 mfma16h(u32x4 a, u32x4 b, f32x4 c) {
  return __builtin_amdgcn_mfma_f32_16x16x32_f16(__builtin_bit_cast(f16x8, a), __builtin_bit_cast(f16x8, b), c, 0, 0, 0);
}
DEV void split2(float v0, float v1, u32& hi, u32& lo) {
  hi = pk_bf16(v0, v1);
  lo = pk_bf16(v0 - lo_f(hi), v1 - hi_f(hi));
}

DEV bf16x8 tr_frag(const char* p, int d2) {
  s16x4 a0 = tr_read(p);
  s16x4 a1 = tr_read(p + d2);
  return (bf16x8){a0[0], a0[1], a0[2], a0[3], a1[0], a1[1], a1[2], a1[3]};
}

template <int NH>
DEV void gemm1_loop(char* smem, int bj, int xadr0, const int (&adr)[4], int adrc,
                    f32x4 (&acc)[2][4], f32x4& cs, f32x4 (&sm)[2], f32x4 (&sq)[2]) {
  const short one = 0x3f80;
  const bf16x8 ones = {one, one, one, one, one, one, one, one};
  constexpr int NT = NH ? 3 : 4;
  bf16x8 xf[2][2], a[2][NT], ac[2];
  auto rd = [&](int u, int slot) {
    const int s = u >> 1, k2 = u & 1;
    const char* xb = smem + (s & 3) * 32768;
    const char* wb = xb + 16384 + k2 * 8192;
    xf[slot][0] = *(const bf16x8*)(xb + (xadr0 ^ (64 * k2)));
    xf[slot][1] = *(const bf16x8*)(xb + (xadr0 ^ (64 * k2)) + 2048);
#pragma unroll
    for (int T = 0; T < NT; ++T) a[slot][T] = tr_frag(wb + adr[T], 1024);
    ac[slot] = tr_frag(wb + adrc, 1024);
  };
  CFENCE(); __builtin_amdgcn_s_barrier(); CFENCE();
  rd(0, 0);
#pragma unroll
  for (int u = 0; u < 16; ++u) {
    const int c = u & 1;
    if (u + 1 < 16) {
      if (u & 1) {
        RAW_BAR();
        STAMP(0, 4 + (u >> 1));
      }
      rd(u + 1, c ^ 1);
    }
#pragma unroll
    for (int T = 0; T < NT; ++T) {
      acc[0][T] = mfma16(a[c][T], xf[c][0], acc[0][T]);
      acc[1][T] = mfma16(a[c][T], xf[c][1], acc[1][T]);
    }
    cs = mfma16(ac[c], ones, cs);
    if (NH) {
      sm[0] = mfma16(ones, xf[c][0], sm[0]);
      sq[0] = mfma16(xf[c][0], xf[c][0], sq[0]);
      sm[1] = mfma16(ones, xf[c][1], sm[1]);
      sq[1] = mfma16(xf[c][1], xf[c][1], sq[1]);
    }
    if (u + 1 < 16) {
#pragma unroll
      for (int i = 0; i < 7; ++i) {
        __builtin_amdgcn_sched_group_barrier(0x8, 1, 0);
        __builtin_amdgcn_sched_group_barrier(0x100, 2, 0);
      }
    }
    __builtin_amdgcn_sched_barrier(0);
  }
}

template <int NH>
DEV void gemm1_epi(const float* cs_lds, const float* st_lds, const u32x2 (&b1v)[4],
                   u16* __restrict__ h, int t0, int n0, int wt, int r, int g, f32x4 (&acc)[2][4]) {
  constexpr int NT = NH ? 3 : 4;
  constexpr int TB = NH ? 4 : 0;
  float mean_b[2], inv_s[2];
#pragma unroll
  for (int tt = 0; tt < 2; ++tt) {
    f2_t st = *(const f2_t*)(st_lds + 2 * (32 * wt + 16 * tt + r));
    mean_b[tt] = st[0];
    inv_s[tt] = 1.0f / st[1];
  }
  u32x2 hp[2][NT];
#pragma unroll
  for (int T = 0; T < NT; ++T) {
    f32x4 cs = *(const f32x4*)(cs_lds + 16 * (TB + T) + 4 * g);
    u32x2 bb = b1v[T];
    f32x4 bv = {lo_f(bb[0]), hi_f(bb[0]), lo_f(bb[1]), hi_f(bb[1])};
#pragma unroll
    for (int tt = 0; tt < 2; ++tt) {
      const float ms = mean_b[tt] * inv_s[tt];
      f32x4 v = acc[tt][T] * inv_s[tt] + (bv - cs * ms);
      const float c1 = -2.0f * 0.7978845608028654f * 1.4426950408889634f;
      const float c2 = c1 * 0.044715f;
      f32x4 z = v * (v * v * c2 + c1);
      f32x4 d;
#pragma unroll
      for (int i = 0; i < 4; ++i) d[i] = __builtin_amdgcn_exp2f(z[i]);
      d = d + 1.0f;
#pragma unroll
      for (int i = 0; i < 4; ++i) d[i] = __builtin_amdgcn_rcpf(d[i]);
      v = v * d;
      hp[tt][T][0] = pk_bf16(v[0], v[1]);
      hp[tt][T][1] = pk_bf16(v[2], v[3]);
    }
  }
#pragma unroll
  for (int tt = 0; tt < 2; ++tt) {
    u16* hrow = h + (size_t)(t0 + 32 * wt + 16 * tt + r) * F + n0 + 16 * TB;
#pragma unroll
    for (int T = 0; T + 1 < NT; T += 2) {
      auto s0 = __builtin_amdgcn_permlane16_swap(hp[tt][T][0], hp[tt][T + 1][0], false, false);
      auto s1 = __builtin_amdgcn_permlane16_swap(hp[tt][T][1], hp[tt][T + 1][1], false, false);
      u32x4 o = {s0[0], s1[0], s0[1], s1[1]};
      HSTORE4((u32x4*)(hrow + 16 * (T + (g & 1)) + 8 * (g >> 1)), o);
    }
    if (NH) HSTORE2((u32x2*)(hrow + 32 + 4 * g), hp[tt][2]);
  }
}

template <int WHICH>
DEV void k1_loader(char* smem, const u16* __restrict__ x, const u16* __restrict__ w1, int t0, int n0, int l,
                   int bj, u16* __restrict__ xnT, const u16* __restrict__ w2pf) {
  const u16* src[2];
  bool ok[2];
  if (WHICH == 0) {
#pragma unroll
    for (int v = 0; v < 2; ++v) {
      src[v] = x + (size_t)(t0 + (l >> 3)) * D + 8 * ((l & 7) ^ ((l >> 4) + 4 * v));
      ok[v] = true;
    }
  } else {
#pragma unroll
    for (int v = 0; v < 2; ++v) {
      const int c16 = ((l & 15) - 2 * ((l >> 4) + 4 * v)) & 15;
      ok[v] = c16 < 14;
      src[v] = w1 + (size_t)(l >> 4) * F + n0 + c16 * 8;
    }
  }
  auto stage = [&](int s) {
    char* dst = smem + (s & 3) * 32768 + WHICH * 16384;
#pragma unroll
    for (int i = 0; i < 16; ++i) {
      if (WHICH == 0) {
        GLDS16(src[i & 1] + (size_t)(8 * i) * D + 64 * s, dst + i * 1024);
      } else {
        const int v = (i >> 1) & 1;
        if (ok[v]) GLDS16(src[v] + (size_t)(64 * s + 4 * i) * F, dst + i * 1024);
      }
    }
  };
  stage(0); stage(1); stage(2); stage(3);
#pragma unroll
  for (int i = 0; i < 4; ++i) GLDS16(w2pf + (i == 3 ? 1280 : 512 * i) + 8 * l, smem + K1_PF + i * 1024);
  CFENCE(); __builtin_amdgcn_s_barrier(); CFENCE();
  WAIT_V(52);
  __builtin_amdgcn_s_barrier(); CFENCE();
#pragma unroll
  for (int s = 1; s < 8; ++s) {
    if (s <= 3) WAIT_V(36);
    else if (s <= 5) WAIT_V(32);
    else if (s == 6) WAIT_V(16);
    else WAIT_V(0);
    __builtin_amdgcn_s_barrier(); CFENCE();
    if (s + 3 < 8) stage(s + 3);
  }
  u32x4 xs[4];
#pragma unroll
  for (int i = 0; i < 4; ++i)
    xs[i] = *(const u32x4*)(x + (size_t)(t0 + 64 * WHICH + 16 * i + (l >> 2)) * D + 32 * bj + 8 * (l & 3));
  CFENCE(); __builtin_amdgcn_s_barrier(); CFENCE();
  const float* st_lds = (const float*)(smem + K1_CS + 512);
  u16* tb = (u16*)(smem + K1_KP + WHICH * 8192);
#pragma unroll
  for (int i = 0; i < 4; ++i) {
    const int tl = 16 * i + (l >> 2);
    f2_t st = *(const f2_t*)(st_lds + 2 * (64 * WHICH + tl));
#pragma unroll
    for (int e = 0; e < 4; ++e) {
      float v0 = rbf(lo_f(xs[i][e]) - st[0]) / st[1];
      float v1 = rbf(hi_f(xs[i][e]) - st[0]) / st[1];
      u32 pk = pk_bf16(v0, v1);
      tb[(8 * (l & 3) + 2 * e) * 72 + tl] = (u16)(pk & 0xffffu);
      tb[(8 * (l & 3) + 2 * e + 1) * 72 + tl] = (u16)(pk >> 16);
    }
  }
  LGKM0();
  CFENCE();
#pragma unroll
  for (int i = 0; i < 4; ++i) {
    const int dd = 8 * i + (l >> 3), ck = l & 7;
    u32x4 o = *(const u32x4*)(tb + dd * 72 + 8 * ck);
    *(u32x4*)(xnT + (size_t)(32 * bj + dd) * L + t0 + 64 * WHICH + 8 * ck) = o;
  }
}

__global__ __launch_bounds__(640, 3) void k1(const u16* __restrict__ x, const u16* __restrict__ w1,
                                             const u16* __restrict__ b1, const u16* __restrict__ w2,
                                             const u16* __restrict__ logA, const u16* __restrict__ Aim,
                                             const u16* __restrict__ Cr, const u16* __restrict__ Ci,
                                             const u16* __restrict__ logdt, u16* __restrict__ h,
                                             u16* __restrict__ xnT, u16* __restrict__ Kr,
                                             float* __restrict__ stats, u32* __restrict__ cnt) {
  __shared__ __attribute__((aligned(16))) char smem[K1_SMEM];
  const int b = blockIdx.x;
  const int xcd = b & 7, idx = b >> 3;
  const int bi = 2 * xcd + (idx >> 4), bj = idx & 15;
  const int t0 = bi * 128, n0 = bj * 112;
  const int tid = threadIdx.x, l = tid & 63, r = l & 15, g = l >> 4;
  const int w = __builtin_amdgcn_readfirstlane(tid >> 6);
  if (w == 8) { k1_loader<0>(smem, x, w1, t0, n0, l, bj, xnT, w2 + (size_t)b * 3584); return; }
  if (w == 9) { k1_loader<1>(smem, x, w1, t0, n0, l, bj, xnT, w2 + (size_t)b * 3584 + 1792); return; }
  const int wt = w & 3, nh = w >> 2;
  STAMP(0, 0);
  if (b == 0 && tid < 8) cnt[32 * tid] = 0u;

  u32x2 b1v[4];
#pragma unroll
  for (int T = 0; T < 4; ++T) b1v[T] = *(const u32x2*)(b1 + n0 + 16 * (nh ? (T < 3 ? 4 + T : 6) : T) + 4 * g);
  const u32x4 kix0 = *(const u32x4*)(g_kidx.v + 8 * (tid & 255));
  const u32x4 kix1 = *(const u32x4*)(g_kidx.v + 8 * (256 + (tid & 1)));
  const int cc = w >> 2, ch = 2 * b + cc, qn = w & 3;
  f32x4* Kp = (f32x4*)(smem + K1_KP);
  {
    const int base = ch * NS + 32 * qn + 8 * g;
    bf16x8 vla = *(const bf16x8*)(logA + base);
    bf16x8 vai = *(const bf16x8*)(Aim + base);
    bf16x8 vcr = *(const bf16x8*)(Cr + base);
    bf16x8 vci = *(const bf16x8*)(Ci + base);
    const u32 ldt2 = ((const u32*)logdt)[b];
    const float dt = rbf(expf(cc ? hi_f(ldt2) : lo_f(ldt2)));
    const float pF = (float)(16 * r);
    float a2[8], br[8], cr[8], ci[8];
#pragma unroll
    for (int j = 0; j < 8; ++j) {
      float a = -rbf(__expf(bf2f((u16)vla[j]))) * dt;
      a2[j] = a * 1.4426950408889634f;
      br[j] = (bf2f((u16)vai[j]) * dt) * 0.15915494309189535f;
      cr[j] = bf2f((u16)vcr[j]) * dt;
      ci[j] = bf2f((u16)vci[j]) * dt;
    }
    STAMP(0, 1);
    float mx = 0.f;
#pragma unroll
    for (int j = 0; j < 8; ++j) mx = fmaxf(mx, fabsf(cr[j]) + fabsf(ci[j]));
    mx = fmaxf(mx, __shfl_xor(mx, 16));
    mx = fmaxf(mx, __shfl_xor(mx, 32));
    u32 ex_ = (__float_as_uint(mx) >> 23) & 255u;
    ex_ = ex_ < 1u ? 1u : (ex_ > 253u ? 253u : ex_);
    const float S = __uint_as_float((254u - ex_) << 23);
    const float outS = 2.0f * __uint_as_float(ex_ << 23);
    u32x4 Fr, Fi;
#pragma unroll
    for (int j2 = 0; j2 < 4; ++j2) {
      float fr[2], fi[2];
#pragma unroll
      for (int e = 0; e < 2; ++e) {
        int j = 2 * j2 + e;
        float ex = __builtin_amdgcn_exp2f(a2[j] * pF) * S;
        float an = __builtin_amdgcn_fractf(br[j] * pF);
        float er = ex * __builtin_amdgcn_cosf(an);
        float ei = ex * __builtin_amdgcn_sinf(an);
        fr[e] = cr[j] * er - ci[j] * ei;
        fi[e] = -(cr[j] * ei + ci[j] * er);
      }
      Fr[j2] = pk_f16(fr[0], fr[1]);
      Fi[j2] = pk_f16(fi[0], fi[1]);
    }
#pragma unroll
    for (int rt = 0; rt < 3; ++rt) {
      const float pG = (float)kpos_row(rt, r);
      u32x4 Gr, Gi;
#pragma unroll
      for (int j2 = 0; j2 < 4; ++j2) {
        float gr[2], gi[2];
#pragma unroll
        for (int e = 0; e < 2; ++e) {
          int j = 2 * j2 + e;
          float ex = __builtin_amdgcn_exp2f(a2[j] * pG);
          float an = __builtin_amdgcn_fractf(br[j] * pG);
          gr[e] = ex * __builtin_amdgcn_cosf(an);
          gi[e] = ex * __builtin_amdgcn_sinf(an);
        }
        Gr[j2] = pk_f16(gr[0], gr[1]);
        Gi[j2] = pk_f16(gi[0], gi[1]);
      }
      f32x4 a = {0.f, 0.f, 0.f, 0.f};
      a = mfma16h(Gr, Fr, a);
      a = mfma16h(Gi, Fi, a);
      Kp[(w * 3 + rt) * 64 + l] = a * outS;
    }
  }
  STAMP(0, 2);
  RAW_BAR();
  {
    auto expand = [&](const u32x4& kix, int c2, int chunk) {
      const float* kt = (const float*)(smem + K1_KP) + c2 * 4 * 768;
      float v[8];
#pragma unroll
      for (int e = 0; e < 8; ++e) {
        u32 ix = (e & 1) ? (kix[e >> 1] >> 16) : (kix[e >> 1] & 0xffffu);
        bool z = ix == 0xffffu;
        ix = z ? 0u : ix;
        float kv = (kt[ix] + kt[768 + ix]) + (kt[1536 + ix] + kt[2304 + ix]);
        v[e] = z ? 0.f : kv;
      }
      u32x4 o = {pk_bf16(v[0], v[1]), pk_bf16(v[2], v[3]), pk_bf16(v[4], v[5]), pk_bf16(v[6], v[7])};
      *(u32x4*)(Kr + (size_t)(2 * b + c2) * KRP + 8 * chunk) = o;
    };
    expand(kix0, tid >> 8, tid & 255);
    if (tid < 4) expand(kix1, tid >> 1, 256 + (tid & 1));
  }
  STAMP(0, 3);

  const int q = r >> 2, p = r & 3;
  const int rowoff = (8 * g + q) * 256 + 8 * (p & 1);
  const int prot = (p >> 1) + 2 * q + 8 * (g & 1);
  int adr[4];
#pragma unroll
  for (int T = 0; T < 4; ++T) adr[T] = rowoff + (((2 * (4 * nh + T) + prot) & 15) << 4);
  const int Tc = nh ? (4 + (wt > 2 ? 2 : wt)) : wt;
  const int adrc = rowoff + (((2 * Tc + prot) & 15) << 4);
  const int xadr0 = (32 * wt + r) * 128 + ((g ^ ((r >> 1) & 7)) << 4);

  f32x4 acc[2][4];
#pragma unroll
  for (int tt = 0; tt < 2; ++tt)
#pragma unroll
    for (int T = 0; T < 4; ++T) acc[tt][T] = (f32x4){0.f, 0.f, 0.f, 0.f};
  f32x4 cs = {0.f, 0.f, 0.f, 0.f};
  f32x4 sm[2] = {{0.f, 0.f, 0.f, 0.f}, {0.f, 0.f, 0.f, 0.f}}, sq[2] = {{0.f, 0.f, 0.f, 0.f}, {0.f, 0.f, 0.f, 0.f}};
  float* cs_lds = (float*)(smem + K1_CS);
  float* st_lds = (float*)(smem + K1_CS + 512);
  if (nh == 0) {
    gemm1_loop<0>(smem, bj, xadr0, adr, adrc, acc, cs, sm, sq);
    if (r == 0) *(f32x4*)(cs_lds + 16 * Tc + 4 * g) = cs;
    RAW_BAR();
    STAMP(0, 12);
    gemm1_epi<0>(cs_lds, st_lds, b1v, h, t0, n0, wt, r, g, acc);
    STAMP(0, 13);
  } else {
    gemm1_loop<1>(smem, bj, xadr0, adr, adrc, acc, cs, sm, sq);
    if (r == 0) *(f32x4*)(cs_lds + 16 * Tc + 4 * g) = cs;
#pragma unroll
    for (int tt = 0; tt < 2; ++tt) {
      float cand = (p == 0) ? sq[tt][0] : (p == 1) ? sq[tt][1] : (p == 2) ? sq[tt][2] : sq[tt][3];
      float sumsq = __shfl(cand, r + 16 * q);
      float mean = sm[tt][0] * (1.0f / D);
      float var = fmaxf(sumsq * (1.0f / D) - mean * mean, 0.f);
      float m = rbf(mean), v = rbf(var);
      float s = rbf(sqrtf(rbf(v + 1e-5f)));
      if (g == 0) {
        f2_t st = {m, s};
        *(f2_t*)(st_lds + 2 * (32 * wt + 16 * tt + r)) = st;
        if (bj == 0) *(f2_t*)(stats + 2 * (t0 + 32 * wt + 16 * tt + r)) = st;
      }
    }
    STAMP(0, 11);
    RAW_BAR();
    STAMP(0, 12);
    gemm1_epi<1>(cs_lds, st_lds, b1v, h, t0, n0, wt, r, g, acc);
    STAMP(0, 13);
    STAMP(0, 14);
  }
}

constexpr int CPITCH = 4160;
constexpr int CH_BYTES = 8 * CPITCH + 4608;
constexpr int K2_SMEM = 81920;

DEV u32 funnel16(u32 hi, u32 lo) { return __builtin_amdgcn_alignbyte(hi, lo, 2); }

DEV void conv_role(char* smem, const u16* __restrict__ Kr, const u16* __restrict__ xnT,
                   u16* __restrict__ yT, u32* __restrict__ cnt) {
  const int c = blockIdx.x;
  const int tid = threadIdx.x, l = tid & 63, r = l & 15, g = l >> 4;
  const int w = __builtin_amdgcn_readfirstlane(tid >> 6);
  STAMP(1, 0);
  auto put = [&](const u32x4& ka, const u32x4& kb, char* base) {
    u32 d[8] = {ka[0], ka[1], ka[2], ka[3], kb[0], kb[1], kb[2], kb[3]};
#pragma unroll
    for (int s = 0; s < 8; ++s) {
      const int e = 7 - s;
      u32x4 o;
#pragma unroll
      for (int i = 0; i < 4; ++i)
        o[i] = (e & 1) ? funnel16(d[(e + 1) / 2 + i], d[(e - 1) / 2 + i]) : d[e / 2 + i];
      *(u32x4*)(base + s * CPITCH) = o;
    }
  };
  {
    const int c2 = tid >> 8, ck = tid & 255;
    const u16* kr = Kr + (size_t)(2 * c + c2) * KRP + 8 * ck;
    u32x4 ka = *(const u32x4*)(kr), kb = *(const u32x4*)(kr + 8);
    u32x4 uu = *(const u32x4*)(xnT + (size_t)(2 * c) * L + 8 * tid);
    u32x4 kc, kd;
    if (tid < 2) {
      const u16* kr2 = Kr + (size_t)(2 * c + tid) * KRP + 8 * 256;
      kc = *(const u32x4*)(kr2); kd = *(const u32x4*)(kr2 + 8);
    }
    put(ka, kb, smem + c2 * CH_BYTES + 16 * ck);
    *(u32x4*)(smem + c2 * CH_BYTES + 8 * CPITCH + 512 + 16 * ck) = uu;
    if (tid < 2) put(kc, kd, smem + tid * CH_BYTES + 16 * 256);
    if (ck < 32) *(u32x4*)(smem + c2 * CH_BYTES + 8 * CPITCH + 16 * ck) = (u32x4){0u, 0u, 0u, 0u};
  }
  __syncthreads();
  STAMP(1, 1);
  const int cc = w >> 2, ch = 2 * c + cc, wq = w & 3;
  f32x4 acc[8];
#pragma unroll
  for (int i = 0; i < 8; ++i) acc[i] = (f32x4){0.f, 0.f, 0.f, 0.f};
  {
    const char* base = smem + cc * CH_BYTES;
    const char* ab = base + (r & 7) * CPITCH + 2 * (2048 - 8 * g - 8 * (r >> 3)) - 64 * (2 * wq);
    const char* bb = base + 8 * CPITCH + 2 * (256 + 16 * r - 8 * g + 8) - 64 * (2 * wq);
#pragma unroll
    for (int cl = 0; cl < 2; ++cl) {
      bf16x8 A[8], B[8];
#pragma unroll
      for (int pq = 0; pq < 8; ++pq) {
        A[pq] = *(const bf16x8*)(ab - 64 * cl - 512 * pq);
        B[pq] = *(const bf16x8*)(bb - 64 * cl + 512 * pq);
      }
#pragma unroll
      for (int p = 0; p < 8; ++p)
#pragma unroll
        for (int i = p; i < 8; ++i) acc[i] = mfma16(A[p], B[i - p], acc[i]);
    }
  }
  __syncthreads();
  STAMP(1, 2);
  f32x4* xb = (f32x4*)(smem + cc * CH_BYTES);
#pragma unroll
  for (int i = 0; i < 8; ++i) xb[(wq * 8 + i) * 64 + l] = acc[i];
  __syncthreads();
  {
    u16* yrow = yT + (size_t)ch * L + 16 * r + 4 * g + 512 * wq;
#pragma unroll
    for (int i = 0; i < 2; ++i) {
      const f32x4* p = xb + (2 * wq + i) * 64 + l;
      f32x4 v = (p[0] + p[512]) + (p[1024] + p[1536]);
      u64 o = (u64)pk_bf16(v[0], v[1]) | ((u64)pk_bf16(v[2], v[3]) << 32);
      __hip_atomic_store((u64*)(yrow + 256 * i), o, __ATOMIC_RELAXED, __HIP_MEMORY_SCOPE_AGENT);
    }
  }
  WAIT_V(0);
  __syncthreads();
  STAMP(1, 3);
  if (tid == 0)
    __hip_atomic_fetch_add(cnt + 32 * (c >> 5), 1u, __ATOMIC_RELAXED, __HIP_MEMORY_SCOPE_AGENT);
}

constexpr int G2_NR = 5, G2_NST = 28;

template <int WHICH>
DEV void g2_loader(char* smem, const u16* __restrict__ h, const u16* __restrict__ w2, int t0, int d0, int l,
                   int hf) {
  const u16* src[2];
#pragma unroll
  for (int v = 0; v < 2; ++v) {
    if (WHICH == 0) src[v] = h + (size_t)(t0 + 32 * hf + (l >> 3)) * F + 8 * ((l & 7) ^ ((l >> 4) + 4 * v));
    else src[v] = w2 + (size_t)(32 * hf + (l >> 3)) * D + d0 + 8 * ((l & 7) ^ ((((l >> 4) & 1) | (v << 1)) << 1));
  }
  char* dst0 = smem + WHICH * 8192 + hf * 4096;
  auto stage = [&](int s) {
    char* dst = dst0 + (s % G2_NR) * 16384;
#pragma unroll
    for (int i = 0; i < 4; ++i) {
      if (WHICH == 0) GLDS16(src[i & 1] + (size_t)(8 * i) * F + 64 * s, dst + i * 1024);
      else GLDS16(src[i & 1] + (size_t)(64 * s + 8 * i) * D, dst + i * 1024);
    }
  };
#pragma unroll
  for (int s = 0; s < G2_NR; ++s) stage(s);
  WAIT_V(16);
  __builtin_amdgcn_s_barrier(); CFENCE();
#pragma unroll
  for (int s = 0; s <= G2_NST - 2; ++s) {
    if (s + 4 <= G2_NST - 1) WAIT_V(12);
    else if (s + 3 == G2_NST - 1) WAIT_V(8);
    else if (s + 2 == G2_NST - 1) WAIT_V(4);
    else WAIT_V(0);
    __builtin_amdgcn_s_barrier(); CFENCE();
    if (s + G2_NR < G2_NST) stage(s + G2_NR);
  }
}

DEV void gemm2_role(char* smem, const u16* __restrict__ h, const u16* __restrict__ w2,
                    const u16* __restrict__ b2, const u16* __restrict__ x,
                    const u16* __restrict__ Dsk, const float* __restrict__ stats,
                    const u16* yT, u32* cnt, u16* __restrict__ out) {
  const int b = blockIdx.x - 256;
  const int xcd = b & 7, idx = b >> 3;
  const int ti = 4 * xcd + (idx >> 3), dj = idx & 7;
  const int t0 = 64 * ti, d0 = 64 * dj;
  const int tid = threadIdx.x, l = tid & 63, r = l & 15, g = l >> 4;
  const int w = __builtin_amdgcn_readfirstlane(tid >> 6);
  if (w >= 6) { g2_loader<1>(smem, h, w2, t0, d0, l, w & 1); return; }
  if (w >= 4) { g2_loader<0>(smem, h, w2, t0, d0, l, w & 1); return; }
  const int dh = w >> 1, th = w & 1;
  STAMP(1, 0);
  constexpr int NR = G2_NR, NST = G2_NST;

  const int q = r >> 2, p = r & 3;
  const int fr = ((q >> 1) & 1) | ((g & 1) << 1);
  int aadr[2];
#pragma unroll
  for (int a = 0; a < 2; ++a)
    aadr[a] = 8192 + (8 * g + q) * 128 + (((2 * (2 * dh + a) + (p >> 1)) ^ (fr << 1)) << 4) + 8 * (p & 1);
  const int badr = (32 * th + r) * 128 + ((g ^ ((r >> 1) & 7)) << 4);

  u32x4 xv[2], dv, bv;
  f2_t st[2];
  u64 yv[4];

  f32x4 acc[2][2];
#pragma unroll
  for (int i = 0; i < 2; ++i)
#pragma unroll
    for (int j = 0; j < 2; ++j) acc[i][j] = (f32x4){0.f, 0.f, 0.f, 0.f};

  bf16x8 af[2][2][2], bf[2][2][2];
  auto rd = [&](int s, int slot) {
    const char* sb = smem + (s % NR) * 16384;
#pragma unroll
    for (int k2 = 0; k2 < 2; ++k2) {
      bf[slot][k2][0] = *(const bf16x8*)(sb + (badr ^ (64 * k2)));
      bf[slot][k2][1] = *(const bf16x8*)(sb + (badr ^ (64 * k2)) + 2048);
#pragma unroll
      for (int a = 0; a < 2; ++a) af[slot][k2][a] = tr_frag(sb + aadr[a] + k2 * 4096, 512);
    }
  };
  u16* ytile = (u16*)(smem + ((NST - 5) % NR) * 16384);
  u32 cv = 0;
  bool got = false;
  auto step = [&](int sm10, bool more) {
    const int c = sm10 & 1;
    if (more) {
      RAW_BAR();
      rd(sm10 + 1, c ^ 1);
    }
#pragma unroll
    for (int k2 = 0; k2 < 2; ++k2)
#pragma unroll
      for (int a = 0; a < 2; ++a) {
        acc[a][0] = mfma16(af[c][k2][a], bf[c][k2][0], acc[a][0]);
        acc[a][1] = mfma16(af[c][k2][a], bf[c][k2][1], acc[a][1]);
      }
    if (more) {
#pragma unroll
      for (int i = 0; i < 6; ++i) {
        __builtin_amdgcn_sched_group_barrier(0x8, 1, 0);
        __builtin_amdgcn_sched_group_barrier(0x100, 2, 0);
      }
    }
    __builtin_amdgcn_sched_barrier(0);
  };
  CFENCE(); __builtin_amdgcn_s_barrier(); CFENCE();
  rd(0, 0);
#pragma unroll 1
  for (int s0 = 0; s0 < 20; s0 += 10) {
#pragma unroll
    for (int j = 0; j < 10; ++j) step(j, true);
  }
#pragma unroll
  for (int s = 20; s < NST; ++s) {
    step(s % 10, s + 1 < NST);
    if (s == 20) {
      cv = __hip_atomic_load(cnt + 32 * dj, __ATOMIC_RELAXED, __HIP_MEMORY_SCOPE_AGENT);
      __builtin_amdgcn_sched_barrier(0);
    }
    if (s == 23) {
      got = __builtin_amdgcn_readfirstlane(cv) >= 32u;
      CFENCE();
      int tz = tid;
      asm volatile("" : "+v"(tz));
      if (got) {
#pragma unroll
        for (int i = 0; i < 4; ++i) {
          int id = tz + 256 * i;
          yv[i] = __hip_atomic_load((const u64*)(yT + (size_t)(d0 + (id >> 4)) * L + t0 + 4 * (id & 15)),
                                    __ATOMIC_RELAXED, __HIP_MEMORY_SCOPE_AGENT);
        }
      }
      const int r2 = tz & 15, g2 = (tz >> 4) & 3;
      const int dl2 = 32 * dh + 16 * (g2 & 1) + 8 * (g2 >> 1);
#pragma unroll
      for (int bb = 0; bb < 2; ++bb) {
        const int t = t0 + 32 * th + 16 * bb + r2;
        xv[bb] = *(const u32x4*)(x + (size_t)t * D + d0 + dl2);
        st[bb] = *(const f2_t*)(stats + 2 * t);
      }
      dv = *(const u32x4*)(Dsk + d0 + dl2);
      bv = *(const u32x4*)(b2 + d0 + dl2);
      __builtin_amdgcn_sched_barrier(0);
    }
  }
  STAMP(1, 1);
  {
    int tz = tid;
    asm volatile("" : "+v"(tz));
    if (!got) {
      u32 spins = 0;
      while (__hip_atomic_load(cnt + 32 * dj, __ATOMIC_RELAXED, __HIP_MEMORY_SCOPE_AGENT) < 32u) {
        __builtin_amdgcn_s_sleep(2);
        if (++spins > (1u << 24)) break;
      }
      CFENCE();
#pragma unroll
      for (int i = 0; i < 4; ++i) {
        int id = tz + 256 * i;
        yv[i] = __hip_atomic_load((const u64*)(yT + (size_t)(d0 + (id >> 4)) * L + t0 + 4 * (id & 15)),
                                  __ATOMIC_RELAXED, __HIP_MEMORY_SCOPE_AGENT);
      }
    }
#pragma unroll
    for (int i = 0; i < 4; ++i) {
      int id = tz + 256 * i;
      *(u64*)(ytile + (id >> 4) * 72 + 4 * (id & 15)) = yv[i];
    }
    RAW_BAR();
    STAMP(1, 2);
    const int r2 = tz & 15, g2 = (tz >> 4) & 3;
    const int dl2 = 32 * dh + 16 * (g2 & 1) + 8 * (g2 >> 1);
#pragma unroll
    for (int bb = 0; bb < 2; ++bb) {
      float v[8];
#pragma unroll
      for (int i = 0; i < 4; ++i) {
        auto sw = __builtin_amdgcn_permlane16_swap(__float_as_uint(acc[0][bb][i]),
                                                   __float_as_uint(acc[1][bb][i]), false, false);
        v[i] = __uint_as_float(sw[0]);
        v[4 + i] = __uint_as_float(sw[1]);
      }
      const int tl = 32 * th + 16 * bb + r2;
      const u16* yp = ytile + dl2 * 72 + tl;
      const float inv_s = 1.0f / st[bb][1];
      float o[8];
#pragma unroll
      for (int e = 0; e < 8; ++e) {
        float xs = (e & 1) ? hi_f(xv[bb][e >> 1]) : lo_f(xv[bb][e >> 1]);
        float ds = (e & 1) ? hi_f(dv[e >> 1]) : lo_f(dv[e >> 1]);
        float bs = (e & 1) ? hi_f(bv[e >> 1]) : lo_f(bv[e >> 1]);
        float y = bf2f(yp[e * 72]);
        float xn = (xs - st[bb][0]) * inv_s;
        o[e] = (xs + (y + xn * ds)) + (v[e] + bs);
      }
      u32x4 ov = {pk_bf16(o[0], o[1]), pk_bf16(o[2], o[3]), pk_bf16(o[4], o[5]), pk_bf16(o[6], o[7])};
      *(u32x4*)(out + (size_t)(t0 + tl) * D + d0 + dl2) = ov;
    }
  }
  STAMP(1, 3);
}

__global__ __launch_bounds__(512, 4) void k2(const u16* __restrict__ Kr, const u16* __restrict__ xnT,
                                             u16* yT, u32* cnt, const u16* __restrict__ h,
                                             const u16* __restrict__ w2, const u16* __restrict__ b2,
                                             const u16* __restrict__ x, const u16* __restrict__ Dsk,
                                             const float* __restrict__ stats, u16* __restrict__ out) {
  __shared__ __attribute__((aligned(16))) char smem[K2_SMEM];
  if (blockIdx.x < 256) conv_role(smem, Kr, xnT, yT, cnt);
  if (blockIdx.x >= 256) gemm2_role(smem, h, w2, b2, x, Dsk, stats, yT, cnt, out);
}

extern "C" void kernel_launch(void* const* d_in, const int* in_sizes, int n_in, void* d_out,
                              int out_size, void* d_ws, size_t ws_size, hipStream_t stream) {
  const u16* x = (const u16*)d_in[0];
  const u16* logA = (const u16*)d_in[1];
  const u16* Aim = (const u16*)d_in[2];
  const u16* Cr = (const u16*)d_in[3];
  const u16* Ci = (const u16*)d_in[4];
  const u16* logdt = (const u16*)d_in[5];
  const u16* Dsk = (const u16*)d_in[6];
  const u16* w1 = (const u16*)d_in[8];
  const u16* b1 = (const u16*)d_in[9];
  const u16* w2 = (const u16*)d_in[10];
  const u16* b2 = (const u16*)d_in[11];
  char* ws = (char*)d_ws;
  u32* cnt = (u32*)ws; ws += 4096;
  float* stats = (float*)ws; ws += L * 2 * 4;
  u16* h = (u16*)ws; ws += (size_t)L * F * 2;
  u16* xnT = (u16*)ws; ws += (size_t)D * L * 2;
  u16* yT = (u16*)ws; ws += (size_t)D * L * 2;
  u16* Kr = (u16*)ws; ws += (size_t)D * KRP * 2;
  k1<<<256, 640, 0, stream>>>(x, w1, b1, w2, logA, Aim, Cr, Ci, logdt, h, xnT, Kr, stats, cnt);
  k2<<<512, 512, 0, stream>>>(Kr, xnT, yT, cnt, h, w2, b2, x, Dsk, stats, (u16*)d_out);
}
```

```cpp
#include <hip/hip_runtime.h>
#include <hip/hip_bf16.h>
#include <stdint.h>

typedef unsigned short u16;
typedef unsigned int u32;
typedef unsigned long long u64;
typedef __attribute__((ext_vector_type(8))) short bf16x8;
typedef __attribute__((ext_vector_type(4))) short s16x4;
typedef __attribute__((ext_vector_type(4))) float f32x4;
typedef __attribute__((ext_vector_type(4))) unsigned int u32x4;
typedef __attribute__((ext_vector_type(2))) unsigned int u32x2;
typedef __attribute__((ext_vector_type(2))) __bf16 bf2_t;
typedef __attribute__((ext_vector_type(2))) float f2_t;

#define DEV __device__ __forceinline__

constexpr int L = 2048, D = 512, NS = 128, F = 1792;
constexpr int KRP = 2064;

DEV float bf2f(u16 v) { return __uint_as_float(((u32)v) << 16); }
DEV u32 pk_bf16(float lo, float hi) {
  f2_t v = {lo, hi};
  bf2_t r = __builtin_convertvector(v, bf2_t);
  return __builtin_bit_cast(u32, r);
}
DEV float lo_f(u32 p) { return __uint_as_float(p << 16); }
DEV float hi_f(u32 p) { return __uint_as_float(p & 0xffff0000u); }
DEV float rbf(float f) { return lo_f(pk_bf16(f, f)); }

#define STAMP(kid, i)
#define STAMPL(i)
#define WAIT_V(n) asm volatile("s_waitcnt vmcnt(" #n ")" ::: "memory")
#define CFENCE() asm volatile("" ::: "memory")

DEV f32x4 mfma16(bf16x8 a, bf16x8 b, f32x4 c) {
  return __builtin_amdgcn_mfma_f32_16x16x32_bf16(a, b, c, 0, 0, 0);
}

typedef __attribute__((address_space(3))) s16x4 lds_s16x4;
DEV s16x4 tr_read(const char* p) {
  return __builtin_amdgcn_ds_read_tr16_b64_v4i16((lds_s16x4*)p);
}

constexpr int K1_RING = 131072;
constexpr int K1_KP = K1_RING;
constexpr int K1_CS = K1_KP + 24576;
constexpr int K1_PF = K1_CS + 512 + 1024;
constexpr int K1_SMEM = K1_PF + 4096;

#define GLDS16(src, dst)                                                                     \
  __builtin_amdgcn_global_load_lds((const __attribute__((address_space(1))) void*)(src),     \
                                   (__attribute__((address_space(3))) void*)(dst), 16, 0, 0)
#define LGKM0() __builtin_amdgcn_s_waitcnt(0xC07F)
#define RAW_BAR() do { LGKM0(); CFENCE(); __builtin_amdgcn_s_barrier(); CFENCE(); } while (0)

#define HSTORE4(p, v) (*(p) = (v))
#define HSTORE2(p, v) (*(p) = (v))
DEV float gelu_tanh(float v) {
  const float c1 = -2.0f * 0.7978845608028654f * 1.4426950408889634f;
  const float c2 = c1 * 0.044715f;
  float u = v * v;
  float z = v * __builtin_fmaf(u, c2, c1);
  float e = __builtin_amdgcn_exp2f(z);
  return v * __builtin_amdgcn_rcpf(1.0f + e);
}

DEV int kpos_row(int rt, int row) {
  if (rt == 0) return row;
  if (rt == 1) return (row < 8) ? (256 + 2 * row) : (512 + 4 * (row & 3) + 256 * ((row >> 2) & 1));
  return (row < 8) ? (1024 + 8 * (row & 1) + 256 * (row >> 1)) : ((row == 8) ? 2048 : 0);
}
struct KIdxTab { u16 v[KRP]; };
constexpr int c_rne_bf16(int l) {
  if (l < 256) return l;
  int sh = (l < 512) ? 1 : (l < 1024) ? 2 : 3;
  return ((l + (1 << (sh - 1)) - 1 + ((l >> sh) & 1)) >> sh) << sh;
}
constexpr int c_kpos_index(int p) {
  int rt = 0, row = 0, lo = 0;
  if (p < 256) { rt = 0; row = p & 15; lo = p >> 4; }
  else if (p < 512) { int q = p - 256; rt = 1; row = (q & 15) >> 1; lo = q >> 4; }
  else if (p < 1024) { int q = p - 512; rt = 1; row = 8 + ((q & 15) >> 2) + 4 * (q >> 8); lo = (q & 255) >> 4; }
  else if (p < 2048) { int q = p - 1024; rt = 2; row = ((q & 15) >> 3) + 2 * (q >> 8); lo = (q & 255) >> 4; }
  else { rt = 2; row = 8; lo = 0; }
  return (rt * 64 + lo + 16 * (row >> 2)) * 4 + (row & 3);
}
constexpr KIdxTab make_kidx() {
  KIdxTab t{};
  for (int y = 0; y < KRP; ++y) {
    int l = 2047 - y;
    t.v[y] = (l < 0) ? (u16)0xFFFF : (u16)c_kpos_index(c_rne_bf16(l));
  }
  return t;
}
__device__ __attribute__((aligned(16))) const KIdxTab g_kidx = make_kidx();

DEV int kpos_index(int p) {
  int rt, row, lo;
  if (p < 256) { rt = 0; row = p & 15; lo = p >> 4; }
  else if (p < 512) { int q = p - 256; rt = 1; row = (q & 15) >> 1; lo = q >> 4; }
  else if (p < 1024) { int q = p - 512; rt = 1; row = 8 + ((q & 15) >> 2) + 4 * (q >> 8); lo = (q & 255) >> 4; }
  else if (p < 2048) { int q = p - 1024; rt = 2; row = ((q & 15) >> 3) + 2 * (q >> 8); lo = (q & 255) >> 4; }
  else { rt = 2; row = 8; lo = 0; }
  return (rt * 64 + lo + 16 * (row >> 2)) * 4 + (row & 3);
}

typedef __attribute__((ext_vector_type(8))) _Float16 f16x8;
typedef __attribute__((ext_vector_type(2))) _Float16 h2_t;
DEV u32 pk_f16(float lo, float hi) {
  f2_t v = {lo, hi};
  h2_t r = __builtin_convertvector(v, h2_t);
  return __builtin_bit_cast(u32, r);
}
DEV f32x4 mfma16h(u32x4 a, u32x4 b, f32x4 c) {
  return __builtin_amdgcn_mfma_f32_16x16x32_f16(__builtin_bit_cast(f16x8, a), __builtin_bit_cast(f16x8, b), c, 0, 0, 0);
}
DEV void split2(float v0, float v1, u32& hi, u32& lo) {
  hi = pk_bf16(v0, v1);
  lo = pk_bf16(v0 - lo_f(hi), v1 - hi_f(hi));
}

DEV bf16x8 tr_frag(const char* p, int d2) {
  s16x4 a0 = tr_read(p);
  s16x4 a1 = tr_read(p + d2);
  return (bf16x8){a0[0], a0[1], a0[2], a0[3], a1[0], a1[1], a1[2], a1[3]};
}

#define G1PAT_N 6
#define G1PAT_D 2
template <int NH>
DEV void gemm1_loop(char* smem, int Tl, int xadr0, const int (&adr)[4],
                    f32x4 (&acc)[2][4], f32x4& cs, f32x4 (&sm)[2], f32x4 (&sq)[2]) {
  const short one = 0x3f80;
  const bf16x8 ones = {one, one, one, one, one, one, one, one};
  constexpr int NT = NH ? 3 : 4;
  bf16x8 xf[2][2], a[2][NT];
  u32 msk[NT];
#pragma unroll
  for (int T = 0; T < NT; ++T) msk[T] = (Tl == T) ? 0xffffffffu : 0u;
  auto rd = [&](int u, int slot) {
    const int s = u >> 1, k2 = u & 1;
    const char* xb = smem + (s & 3) * 32768;
    const char* wb = xb + 16384 + k2 * 8192;
    xf[slot][0] = *(const bf16x8*)(xb + (xadr0 ^ (64 * k2)));
    xf[slot][1] = *(const bf16x8*)(xb + (xadr0 ^ (64 * k2)) + 2048);
#pragma unroll
    for (int T = 0; T < NT; ++T) a[slot][T] = tr_frag(wb + adr[T], 1024);
  };
  auto unit = [&](int u, bool more) {
    const int c = u & 1;
    if (more) {
      if (u & 1) RAW_BAR();
      rd((u + 1) & 7, c ^ 1);
    }
    if (NH) {
      sm[0] = mfma16(ones, xf[c][0], sm[0]);
      sq[0] = mfma16(xf[c][0], xf[c][0], sq[0]);
      sm[1] = mfma16(ones, xf[c][1], sm[1]);
      sq[1] = mfma16(xf[c][1], xf[c][1], sq[1]);
    }
    {
      u32x4 ac = __builtin_bit_cast(u32x4, a[c][0]) & msk[0];
#pragma unroll
      for (int T = 1; T < NT; ++T) ac |= __builtin_bit_cast(u32x4, a[c][T]) & msk[T];
      cs = mfma16(__builtin_bit_cast(bf16x8, ac), ones, cs);
    }
#pragma unroll
    for (int T = 0; T < NT; ++T) {
      acc[0][T] = mfma16(a[c][T], xf[c][0], acc[0][T]);
      acc[1][T] = mfma16(a[c][T], xf[c][1], acc[1][T]);
    }
    if (more) {
#pragma unroll
      for (int i = 0; i < G1PAT_N; ++i) {
        __builtin_amdgcn_sched_group_barrier(0x8, 1, 0);
        __builtin_amdgcn_sched_group_barrier(0x100, G1PAT_D, 0);
      }
    }
    __builtin_amdgcn_sched_barrier(0);
  };
  CFENCE(); __builtin_amdgcn_s_barrier(); CFENCE();
  rd(0, 0);
#pragma unroll
  for (int u = 0; u < 16; ++u) unit(u & 7, u + 1 < 16);
}

template <int NH>
DEV void gemm1_epi(const float* cs_lds, const float* st_lds, const u32x2 (&b1v)[4],
                   u16* __restrict__ h, int t0, int n0, int wt, int r, int g, f32x4 (&acc)[2][4]) {
  constexpr int NT = NH ? 3 : 4;
  constexpr int TB = NH ? 4 : 0;
  float mean_b[2], inv_s[2];
#pragma unroll
  for (int tt = 0; tt < 2; ++tt) {
    f2_t st = *(const f2_t*)(st_lds + 2 * (32 * wt + 16 * tt + r));
    mean_b[tt] = st[0];
    inv_s[tt] = 1.0f / st[1];
  }
  u32x2 hp[2][NT];
#pragma unroll
  for (int T = 0; T < NT; ++T) {
    f32x4 cs = *(const f32x4*)(cs_lds + 16 * (TB + T) + 4 * g);
    u32x2 bb = b1v[T];
    f32x4 bv = {lo_f(bb[0]), hi_f(bb[0]), lo_f(bb[1]), hi_f(bb[1])};
#pragma unroll
    for (int tt = 0; tt < 2; ++tt) {
      const float ms = mean_b[tt] * inv_s[tt];
      f32x4 v = acc[tt][T] * inv_s[tt] + (bv - cs * ms);
      const float c1 = -2.0f * 0.7978845608028654f * 1.4426950408889634f;
      const float c2 = c1 * 0.044715f;
      f32x4 z = v * (v * v * c2 + c1);
      f32x4 d;
#pragma unroll
      for (int i = 0; i < 4; ++i) d[i] = __builtin_amdgcn_exp2f(z[i]);
      d = d + 1.0f;
#pragma unroll
      for (int i = 0; i < 4; ++i) d[i] = __builtin_amdgcn_rcpf(d[i]);
      v = v * d;
      hp[tt][T][0] = pk_bf16(v[0], v[1]);
      hp[tt][T][1] = pk_bf16(v[2], v[3]);
    }
  }
#pragma unroll
  for (int tt = 0; tt < 2; ++tt) {
    u16* hrow = h + (size_t)(t0 + 32 * wt + 16 * tt + r) * F + n0 + 16 * TB;
#pragma unroll
    for (int T = 0; T + 1 < NT; T += 2) {
      auto s0 = __builtin_amdgcn_permlane16_swap(hp[tt][T][0], hp[tt][T + 1][0], false, false);
      auto s1 = __builtin_amdgcn_permlane16_swap(hp[tt][T][1], hp[tt][T + 1][1], false, false);
      u32x4 o = {s0[0], s1[0], s0[1], s1[1]};
      HSTORE4((u32x4*)(hrow + 16 * (T + (g & 1)) + 8 * (g >> 1)), o);
    }
    if (NH) HSTORE2((u32x2*)(hrow + 32 + 4 * g), hp[tt][2]);
  }
}

template <int OP>
DEV void k1_loader(char* smem, const u16* __restrict__ x, const u16* __restrict__ w1, int t0, int n0, int l,
                   int hf, int bj, int bi1, u16* __restrict__ xnT, const u16* __restrict__ w2pf,
                   f32x4* __restrict__ Ktab) {
  const u16* src[2];
#pragma unroll
  for (int v = 0; v < 2; ++v) {
    if (OP == 0) {
      src[v] = x + (size_t)(t0 + 64 * hf + (l >> 3)) * D + 8 * ((l & 7) ^ ((l >> 4) + 4 * v));
    } else {
      int c16 = ((l & 15) - 2 * ((l >> 4) + 4 * v)) & 15;
      c16 = c16 < 14 ? c16 : c16 - 14;
      src[v] = w1 + (size_t)(32 * hf + (l >> 4)) * F + n0 + c16 * 8;
    }
  }
  char* dst0 = smem + OP * 16384 + hf * 8192;
  auto stage = [&](int s) {
    char* dst = dst0 + (s & 3) * 32768;
#pragma unroll
    for (int j = 0; j < 8; ++j) {
      if (OP == 0) GLDS16(src[j & 1] + (size_t)(8 * j) * D + 64 * s, dst + j * 1024);
      else GLDS16(src[(j >> 1) & 1] + (size_t)(64 * s + 4 * j) * F, dst + j * 1024);
    }
  };
  stage(0); stage(1); stage(2); stage(3);
  {
    const int lw_ = 2 * OP + hf;
    const int c14 = (l & 15) < 14 ? (l & 15) : 0;
    const u16* wp = w1 + (size_t)(256 + 128 * bi1 + 32 * lw_ + (l >> 4)) * F + n0 + 8 * c14;
#pragma unroll
    for (int j = 0; j < 8; ++j) GLDS16(wp + (size_t)(4 * j) * F, smem + K1_PF + (j & 3) * 1024);
    GLDS16(x + (size_t)(t0 + 8 * bj + 2 * lw_ + (l >> 5)) * D + 256 + 8 * (l & 31), smem + K1_PF);
  }
  GLDS16(w2pf + 8 * l, smem + K1_PF);
  GLDS16(w2pf + 384 + 8 * l, smem + K1_PF + 1024);
  STAMPL(0);
  WAIT_V(35);
  STAMPL(1);
  __builtin_amdgcn_s_barrier(); CFENCE();
  {
    const f32x4* Kp = (const f32x4*)(smem + K1_KP);
    const int id0 = 96 * (2 * OP + hf) + l;
#pragma unroll
    for (int i = 0; i < 2; ++i) {
      const int id = id0 + 64 * i;
      if (l + 64 * i < 96) {
        const int c3 = id / 192, e = id % 192;
        f32x4 v = (Kp[(4 * c3) * 192 + e] + Kp[(4 * c3 + 1) * 192 + e]) +
                  (Kp[(4 * c3 + 2) * 192 + e] + Kp[(4 * c3 + 3) * 192 + e]);
        Ktab[id] = v;
      }
    }
    LGKM0();
  }
#pragma unroll
  for (int s = 1; s < 8; ++s) {
    if (s <= 3) WAIT_V(29);
    else if (s <= 5) WAIT_V(16);
    else if (s == 6) WAIT_V(8);
    else WAIT_V(0);
    STAMPL(2 * s);
    __builtin_amdgcn_s_barrier(); CFENCE();
    STAMPL(2 * s + 1);
    if (s + 3 < 8) stage(s + 3);
  }
  const int lw = 2 * OP + hf;
  u32x4 xs[2];
#pragma unroll
  for (int i = 0; i < 2; ++i)
    xs[i] = *(const u32x4*)(x + (size_t)(t0 + 32 * lw + 16 * i + (l >> 2)) * D + 32 * bj + 8 * (l & 3));
  CFENCE(); __builtin_amdgcn_s_barrier(); CFENCE();
  const float* st_lds = (const float*)(smem + K1_CS + 512);
  u16* tb = (u16*)(smem + K1_KP + lw * 4096);
#pragma unroll
  for (int i = 0; i < 2; ++i) {
    const int tl = 16 * i + (l >> 2);
    f2_t st = *(const f2_t*)(st_lds + 2 * (32 * lw + tl));
    const float is = 1.0f / st[1];
#pragma unroll
    for (int e = 0; e < 4; ++e) {
      float v0 = rbf(lo_f(xs[i][e]) - st[0]) * is;
      float v1 = rbf(hi_f(xs[i][e]) - st[0]) * is;
      u32 pk = pk_bf16(v0, v1);
      tb[(8 * (l & 3) + 2 * e) * 40 + tl] = (u16)(pk & 0xffffu);
      tb[(8 * (l & 3) + 2 * e + 1) * 40 + tl] = (u16)(pk >> 16);
    }
  }
  LGKM0();
  CFENCE();
#pragma unroll
  for (int i = 0; i < 2; ++i) {
    const int dd = 16 * i + (l >> 2), ck = l & 3;
    u32x4 o = *(const u32x4*)(tb + dd * 40 + 8 * ck);
    *(u32x4*)(xnT + (size_t)(32 * bj + dd) * L + t0 + 32 * lw + 8 * ck) = o;
  }
}

__global__ __launch_bounds__(768, 3) void k1(const u16* __restrict__ x, const u16* __restrict__ w1,
                                             const u16* __restrict__ b1, const u16* __restrict__ w2,
                                             const u16* __restrict__ logA, const u16* __restrict__ Aim,
                                             const u16* __restrict__ Cr, const u16* __restrict__ Ci,
                                             const u16* __restrict__ logdt, u16* __restrict__ h,
                                             u16* __restrict__ xnT, u16* __restrict__ Kr,
                                             float* __restrict__ stats, u32* __restrict__ cnt) {
  __shared__ __attribute__((aligned(16))) char smem[K1_SMEM];
  const int b = blockIdx.x;
  const int xcd = b & 7, idx = b >> 3;
  const int bi = 2 * xcd + (idx >> 4), bj = idx & 15;
  const int t0 = bi * 128, n0 = bj * 112;
  const int tid = threadIdx.x, l = tid & 63, r = l & 15, g = l >> 4;
  const int w = __builtin_amdgcn_readfirstlane(tid >> 6);
  if (w >= 10) { k1_loader<1>(smem, x, w1, t0, n0, l, w & 1, bj, bi & 1, xnT, w2 + (size_t)b * 3584 + (2 + (w & 1)) * 896, (f32x4*)Kr + (size_t)b * 384); return; }
  if (w >= 8) { k1_loader<0>(smem, x, w1, t0, n0, l, w & 1, bj, bi & 1, xnT, w2 + (size_t)b * 3584 + (w & 1) * 896, (f32x4*)Kr + (size_t)b * 384); return; }
  const int wt = w & 3, nh = w >> 2;
  STAMP(0, 0);
  if (b == 0 && tid < 8) cnt[32 * tid] = 0u;

  u32x2 b1v[4];
#pragma unroll
  for (int T = 0; T < 4; ++T) b1v[T] = *(const u32x2*)(b1 + n0 + 16 * (nh ? (T < 3 ? 4 + T : 6) : T) + 4 * g);
  const int cc = w >> 2, ch = 2 * b + cc, qn = w & 3;
  f32x4* Kp = (f32x4*)(smem + K1_KP);
  u32x4 pfv[5];
  {
    const int base = ch * NS + 32 * qn + 8 * g;
    bf16x8 vla = *(const bf16x8*)(logA + base);
    bf16x8 vai = *(const bf16x8*)(Aim + base);
    bf16x8 vcr = *(const bf16x8*)(Cr + base);
    bf16x8 vci = *(const bf16x8*)(Ci + base);
    const u32 ldt2 = ((const u32*)logdt)[b];
    const float dt = rbf(expf(cc ? hi_f(ldt2) : lo_f(ldt2)));
    const float pF = (float)(16 * r);
    float a2[8], br[8], cr[8], ci[8];
#pragma unroll
    for (int j = 0; j < 8; ++j) {
      float a = -rbf(__expf(bf2f((u16)vla[j]))) * dt;
      a2[j] = a * 1.4426950408889634f;
      br[j] = (bf2f((u16)vai[j]) * dt) * 0.15915494309189535f;
      cr[j] = bf2f((u16)vcr[j]) * dt;
      ci[j] = bf2f((u16)vci[j]) * dt;
    }
    STAMP(0, 1);
    float mx = 0.f;
#pragma unroll
    for (int j = 0; j < 8; ++j) mx = fmaxf(mx, fabsf(cr[j]) + fabsf(ci[j]));
    mx = fmaxf(mx, __shfl_xor(mx, 16));
    mx = fmaxf(mx, __shfl_xor(mx, 32));
    u32 ex_ = (__float_as_uint(mx) >> 23) & 255u;
    ex_ = ex_ < 1u ? 1u : (ex_ > 253u ? 253u : ex_);
    const float S = __uint_as_float((254u - ex_) << 23);
    const float outS = 2.0f * __uint_as_float(ex_ << 23);
    u32x4 Fr, Fi;
#pragma unroll
    for (int j2 = 0; j2 < 4; ++j2) {
      float fr[2], fi[2];
#pragma unroll
      for (int e = 0; e < 2; ++e) {
        int j = 2 * j2 + e;
        float ex = __builtin_amdgcn_exp2f(a2[j] * pF) * S;
        float an = __builtin_amdgcn_fractf(br[j] * pF);
        float er = ex * __builtin_amdgcn_cosf(an);
        float ei = ex * __builtin_amdgcn_sinf(an);
        fr[e] = cr[j] * er - ci[j] * ei;
        fi[e] = -(cr[j] * ei + ci[j] * er);
      }
      Fr[j2] = pk_f16(fr[0], fr[1]);
      Fi[j2] = pk_f16(fi[0], fi[1]);
    }
#pragma unroll
    for (int rt = 0; rt < 3; ++rt) {
      const float pG = (float)kpos_row(rt, r);
      u32x4 Gr, Gi;
#pragma unroll
      for (int j2 = 0; j2 < 4; ++j2) {
        float gr[2], gi[2];
#pragma unroll
        for (int e = 0; e < 2; ++e) {
          int j = 2 * j2 + e;
          float ex = __builtin_amdgcn_exp2f(a2[j] * pG);
          float an = __builtin_amdgcn_fractf(br[j] * pG);
          gr[e] = ex * __builtin_amdgcn_cosf(an);
          gi[e] = ex * __builtin_amdgcn_sinf(an);
        }
        Gr[j2] = pk_f16(gr[0], gr[1]);
        Gi[j2] = pk_f16(gi[0], gi[1]);
      }
      f32x4 a = {0.f, 0.f, 0.f, 0.f};
      a = mfma16h(Gr, Fr, a);
      a = mfma16h(Gi, Fi, a);
      Kp[(w * 3 + rt) * 64 + l] = a * outS;
    }
  }
  STAMP(0, 2);
  LGKM0();
  STAMP(0, 3);

  const int q = r >> 2, p = r & 3;
  const int rowoff = (8 * g + q) * 256 + 8 * (p & 1);
  const int prot = (p >> 1) + 2 * q + 8 * (g & 1);
  int adr[4];
#pragma unroll
  for (int T = 0; T < 4; ++T) adr[T] = rowoff + (((2 * (4 * nh + T) + prot) & 15) << 4);
  const int Tc = nh ? (4 + (wt > 2 ? 2 : wt)) : wt;
  const int xadr0 = (32 * wt + r) * 128 + ((g ^ ((r >> 1) & 7)) << 4);

  f32x4 acc[2][4];
#pragma unroll
  for (int tt = 0; tt < 2; ++tt)
#pragma unroll
    for (int T = 0; T < 4; ++T) acc[tt][T] = (f32x4){0.f, 0.f, 0.f, 0.f};
  f32x4 cs = {0.f, 0.f, 0.f, 0.f};
  f32x4 sm[2] = {{0.f, 0.f, 0.f, 0.f}, {0.f, 0.f, 0.f, 0.f}}, sq[2] = {{0.f, 0.f, 0.f, 0.f}, {0.f, 0.f, 0.f, 0.f}};
  float* cs_lds = (float*)(smem + K1_CS);
  float* st_lds = (float*)(smem + K1_CS + 512);
  if (nh == 0) {
    gemm1_loop<0>(smem, Tc, xadr0, adr, acc, cs, sm, sq);
    if (r == 0) *(f32x4*)(cs_lds + 16 * Tc + 4 * g) = cs;
    STAMP(0, 11);
    RAW_BAR();
    STAMP(0, 12);
    gemm1_epi<0>(cs_lds, st_lds, b1v, h, t0, n0, wt, r, g, acc);
    STAMP(0, 13);
  } else {
    gemm1_loop<1>(smem, Tc - 4, xadr0, adr, acc, cs, sm, sq);
    if (r == 0) *(f32x4*)(cs_lds + 16 * Tc + 4 * g) = cs;
#pragma unroll
    for (int tt = 0; tt < 2; ++tt) {
      float cand = (p == 0) ? sq[tt][0] : (p == 1) ? sq[tt][1] : (p == 2) ? sq[tt][2] : sq[tt][3];
      float sumsq = __shfl(cand, r + 16 * q);
      float mean = sm[tt][0] * (1.0f / D);
      float var = fmaxf(sumsq * (1.0f / D) - mean * mean, 0.f);
      float m = rbf(mean), v = rbf(var);
      float s = rbf(sqrtf(rbf(v + 1e-5f)));
      if (g == 0) {
        f2_t st = {m, s};
        *(f2_t*)(st_lds + 2 * (32 * wt + 16 * tt + r)) = st;
        if (bj == 0) *(f2_t*)(stats + 2 * (t0 + 32 * wt + 16 * tt + r)) = st;
      }
    }
    STAMP(0, 11);
    RAW_BAR();
    STAMP(0, 12);
    gemm1_epi<1>(cs_lds, st_lds, b1v, h, t0, n0, wt, r, g, acc);
    STAMP(0, 13);
    STAMP(0, 14);
  }
}

constexpr int CPITCH = 4160;
constexpr int CH_BYTES = 8 * CPITCH + 4608;
constexpr int K2_SMEM = 81920;

DEV u32 funnel16(u32 hi, u32 lo) { return __builtin_amdgcn_alignbyte(hi, lo, 2); }

DEV void conv_role(char* smem, const u16* __restrict__ Kr, const u16* __restrict__ xnT,
                   u16* __restrict__ yT, u32* __restrict__ cnt) {
  const int c = blockIdx.x;
  const int tid = threadIdx.x, l = tid & 63, r = l & 15, g = l >> 4;
  const int w = __builtin_amdgcn_readfirstlane(tid >> 6);
  STAMP(1, 0);
  {
    const int c2 = tid >> 8, ck = tid & 255;
    float* Kt = (float*)(smem + 2 * CH_BYTES);
    f32x4 kv;
    if (tid < 384) kv = ((const f32x4*)Kr)[(size_t)c * 384 + tid];
    u32x4 uu = *(const u32x4*)(xnT + (size_t)(2 * c) * L + 8 * tid);
    const u32x4 ia = *(const u32x4*)(g_kidx.v + 8 * ck);
    const u32x4 ib = *(const u32x4*)(g_kidx.v + 8 * ck + 8);
    if (tid < 384) ((f32x4*)Kt)[tid] = kv;
    *(u32x4*)(smem + c2 * CH_BYTES + 8 * CPITCH + 512 + 16 * ck) = uu;
    if (ck < 32) *(u32x4*)(smem + c2 * CH_BYTES + 8 * CPITCH + 16 * ck) = (u32x4){0u, 0u, 0u, 0u};
    if (ck == 255) {
#pragma unroll
      for (int s = 0; s < 8; ++s)
        *(u32x4*)(smem + c2 * CH_BYTES + 16 * 256 + s * CPITCH) = (u32x4){0u, 0u, 0u, 0u};
    }
    __syncthreads();
    const float* kt = Kt + c2 * 768;
    char* base = smem + c2 * CH_BYTES + 16 * ck;
    u32 d[8];
#pragma unroll
    for (int e2 = 0; e2 < 8; ++e2) {
      u32 pr = e2 < 4 ? ia[e2 & 3] : ib[e2 & 3];
      u32 i0 = pr & 0xffffu, i1 = pr >> 16;
      float v0 = kt[i0 == 0xffffu ? 0u : i0], v1 = kt[i1 == 0xffffu ? 0u : i1];
      d[e2] = pk_bf16(i0 == 0xffffu ? 0.f : v0, i1 == 0xffffu ? 0.f : v1);
    }
#pragma unroll
    for (int s = 0; s < 8; ++s) {
      const int e = 7 - s;
      u32x4 o;
#pragma unroll
      for (int i = 0; i < 4; ++i)
        o[i] = (e & 1) ? funnel16(d[(e + 1) / 2 + i], d[(e - 1) / 2 + i]) : d[e / 2 + i];
      *(u32x4*)(base + s * CPITCH) = o;
    }
  }
  __syncthreads();
  STAMP(1, 1);
  const int cc = w >> 2, ch = 2 * c + cc, wq = w & 3;
  f32x4 acc[8];
#pragma unroll
  for (int i = 0; i < 8; ++i) acc[i] = (f32x4){0.f, 0.f, 0.f, 0.f};
  {
    const char* base = smem + cc * CH_BYTES;
    const char* ab = base + (r & 7) * CPITCH + 2 * (2048 - 8 * g - 8 * (r >> 3)) - 64 * (2 * wq);
    const char* bb = base + 8 * CPITCH + 2 * (256 + 16 * r - 8 * g + 8) - 64 * (2 * wq);
#pragma unroll
    for (int cl = 0; cl < 2; ++cl) {
      bf16x8 A[8], B[8];
#pragma unroll
      for (int pq = 0; pq < 8; ++pq) {
        A[pq] = *(const bf16x8*)(ab - 64 * cl - 512 * pq);
        B[pq] = *(const bf16x8*)(bb - 64 * cl + 512 * pq);
      }
#pragma unroll
      for (int p = 0; p < 8; ++p)
#pragma unroll
        for (int i = p; i < 8; ++i) acc[i] = mfma16(A[p], B[i - p], acc[i]);
    }
  }
  __syncthreads();
  STAMP(1, 2);
  f32x4* xb = (f32x4*)(smem + cc * CH_BYTES);
#pragma unroll
  for (int i = 0; i < 8; ++i) xb[(wq * 8 + i) * 64 + l] = acc[i];
  __syncthreads();
  {
    u16* yrow = yT + (size_t)ch * L + 16 * r + 4 * g + 512 * wq;
#pragma unroll
    for (int i = 0; i < 2; ++i) {
      const f32x4* p = xb + (2 * wq + i) * 64 + l;
      f32x4 v = (p[0] + p[512]) + (p[1024] + p[1536]);
      u64 o = (u64)pk_bf16(v[0], v[1]) | ((u64)pk_bf16(v[2], v[3]) << 32);
      __hip_atomic_store((u64*)(yrow + 256 * i), o, __ATOMIC_RELAXED, __HIP_MEMORY_SCOPE_AGENT);
    }
  }
  WAIT_V(0);
  __syncthreads();
  STAMP(1, 3);
  if (tid == 0)
    __hip_atomic_fetch_add(cnt + 32 * (c >> 5), 1u, __ATOMIC_RELAXED, __HIP_MEMORY_SCOPE_AGENT);
}

constexpr int G2_NR = 5, G2_NST = 28;

template <int WHICH>
DEV void g2_loader(char* smem, const u16* __restrict__ h, const u16* __restrict__ w2, int t0, int d0, int l,
                   int hf) {
  const u16* src[2];
#pragma unroll
  for (int v = 0; v < 2; ++v) {
    if (WHICH == 0) src[v] = h + (size_t)(t0 + 32 * hf + (l >> 3)) * F + 8 * ((l & 7) ^ ((l >> 4) + 4 * v));
    else src[v] = w2 + (size_t)(32 * hf + (l >> 3)) * D + d0 + 8 * ((l & 7) ^ ((((l >> 4) & 1) | (v << 1)) << 1));
  }
  char* dst0 = smem + WHICH * 8192 + hf * 4096;
  auto stage = [&](int s) {
    char* dst = dst0 + (s % G2_NR) * 16384;
#pragma unroll
    for (int i = 0; i < 4; ++i) {
      if (WHICH == 0) GLDS16(src[i & 1] + (size_t)(8 * i) * F + 64 * s, dst + i * 1024);
      else GLDS16(src[i & 1] + (size_t)(64 * s + 8 * i) * D, dst + i * 1024);
    }
  };
#pragma unroll
  for (int s = 0; s < G2_NR; ++s) stage(s);
  WAIT_V(16);
  __builtin_amdgcn_s_barrier(); CFENCE();
#pragma unroll
  for (int s = 0; s <= G2_NST - 2; ++s) {
    if (s + 4 <= G2_NST - 1) WAIT_V(12);
    else if (s + 3 == G2_NST - 1) WAIT_V(8);
    else if (s + 2 == G2_NST - 1) WAIT_V(4);
    else WAIT_V(0);
    __builtin_amdgcn_s_barrier(); CFENCE();
    if (s + G2_NR < G2_NST) stage(s + G2_NR);
  }
}

DEV void gemm2_role(char* smem, const u16* __restrict__ h, const u16* __restrict__ w2,
                    const u16* __restrict__ b2, const u16* __restrict__ x,
                    const u16* __restrict__ Dsk, const float* __restrict__ stats,
                    const u16* yT, u32* cnt, u16* __restrict__ out) {
  const int b = blockIdx.x - 256;
  const int xcd = b & 7, idx = b >> 3;
  const int ti = 4 * xcd + (idx >> 3), dj = idx & 7;
  const int t0 = 64 * ti, d0 = 64 * dj;
  const int tid = threadIdx.x, l = tid & 63, r = l & 15, g = l >> 4;
  const int w = __builtin_amdgcn_readfirstlane(tid >> 6);
  if (w >= 6) { g2_loader<1>(smem, h, w2, t0, d0, l, w & 1); return; }
  if (w >= 4) { g2_loader<0>(smem, h, w2, t0, d0, l, w & 1); return; }
  const int dh = w >> 1, th = w & 1;
  STAMP(1, 0);
  constexpr int NR = G2_NR, NST = G2_NST;

  const int q = r >> 2, p = r & 3;
  const int fr = ((q >> 1) & 1) | ((g & 1) << 1);
  int aadr[2];
#pragma unroll
  for (int a = 0; a < 2; ++a)
    aadr[a] = 8192 + (8 * g + q) * 128 + (((2 * (2 * dh + a) + (p >> 1)) ^ (fr << 1)) << 4) + 8 * (p & 1);
  const int badr = (32 * th + r) * 128 + ((g ^ ((r >> 1) & 7)) << 4);

  u32x4 xv[2], dv, bv;
  f2_t st[2];
  u64 yv[4];

  f32x4 acc[2][2];
#pragma unroll
  for (int i = 0; i < 2; ++i)
#pragma unroll
    for (int j = 0; j < 2; ++j) acc[i][j] = (f32x4){0.f, 0.f, 0.f, 0.f};

  bf16x8 af[2][2][2], bf[2][2][2];
  auto rd = [&](int s, int slot) {
    const char* sb = smem + (s % NR) * 16384;
#pragma unroll
    for (int k2 = 0; k2 < 2; ++k2) {
      bf[slot][k2][0] = *(const bf16x8*)(sb + (badr ^ (64 * k2)));
      bf[slot][k2][1] = *(const bf16x8*)(sb + (badr ^ (64 * k2)) + 2048);
#pragma unroll
      for (int a = 0; a < 2; ++a) af[slot][k2][a] = tr_frag(sb + aadr[a] + k2 * 4096, 512);
    }
  };
  u16* ytile = (u16*)(smem + ((NST - 5) % NR) * 16384);
  u32 cv = 0;
  bool got = false;
  auto step = [&](int sm10, bool more) {
    const int c = sm10 & 1;
    if (more) {
      RAW_BAR();
      rd(sm10 + 1, c ^ 1);
    }
#pragma unroll
    for (int k2 = 0; k2 < 2; ++k2)
#pragma unroll
      for (int a = 0; a < 2; ++a) {
        acc[a][0] = mfma16(af[c][k2][a], bf[c][k2][0], acc[a][0]);
        acc[a][1] = mfma16(af[c][k2][a], bf[c][k2][1], acc[a][1]);
      }
    if (more) {
#pragma unroll
      for (int i = 0; i < 6; ++i) {
        __builtin_amdgcn_sched_group_barrier(0x8, 1, 0);
        __builtin_amdgcn_sched_group_barrier(0x100, 2, 0);
      }
    }
    __builtin_amdgcn_sched_barrier(0);
  };
  CFENCE(); __builtin_amdgcn_s_barrier(); CFENCE();
  rd(0, 0);
#pragma unroll 1
  for (int s0 = 0; s0 < 20; s0 += 10) {
#pragma unroll
    for (int j = 0; j < 10; ++j) step(j, true);
  }
#pragma unroll
  for (int s = 20; s < NST; ++s) {
    step(s % 10, s + 1 < NST);
    if (s == 20) {
      cv = __hip_atomic_load(cnt + 32 * dj, __ATOMIC_RELAXED, __HIP_MEMORY_SCOPE_AGENT);
      __builtin_amdgcn_sched_barrier(0);
    }
    if (s == 23) {
      got = __builtin_amdgcn_readfirstlane(cv) >= 32u;
      CFENCE();
      int tz = tid;
      asm volatile("" : "+v"(tz));
      if (got) {
#pragma unroll
        for (int i = 0; i < 4; ++i) {
          int id = tz + 256 * i;
          yv[i] = __hip_atomic_load((const u64*)(yT + (size_t)(d0 + (id >> 4)) * L + t0 + 4 * (id & 15)),
                                    __ATOMIC_RELAXED, __HIP_MEMORY_SCOPE_AGENT);
        }
      }
      const int r2 = tz & 15, g2 = (tz >> 4) & 3;
      const int dl2 = 32 * dh + 16 * (g2 & 1) + 8 * (g2 >> 1);
#pragma unroll
      for (int bb = 0; bb < 2; ++bb) {
        const int t = t0 + 32 * th + 16 * bb + r2;
        xv[bb] = *(const u32x4*)(x + (size_t)t * D + d0 + dl2);
        st[bb] = *(const f2_t*)(stats + 2 * t);
      }
      dv = *(const u32x4*)(Dsk + d0 + dl2);
      bv = *(const u32x4*)(b2 + d0 + dl2);
      __builtin_amdgcn_sched_barrier(0);
    }
  }
  STAMP(1, 1);
  {
    int tz = tid;
    asm volatile("" : "+v"(tz));
    if (!got) {
      u32 spins = 0;
      while (__hip_atomic_load(cnt + 32 * dj, __ATOMIC_RELAXED, __HIP_MEMORY_SCOPE_AGENT) < 32u) {
        __builtin_amdgcn_s_sleep(2);
        if (++spins > (1u << 24)) break;
      }
      CFENCE();
#pragma unroll
      for (int i = 0; i < 4; ++i) {
        int id = tz + 256 * i;
        yv[i] = __hip_atomic_load((const u64*)(yT + (size_t)(d0 + (id >> 4)) * L + t0 + 4 * (id & 15)),
                                  __ATOMIC_RELAXED, __HIP_MEMORY_SCOPE_AGENT);
      }
    }
#pragma unroll
    for (int i = 0; i < 4; ++i) {
      int id = tz + 256 * i;
      *(u64*)(ytile + (id >> 4) * 72 + 4 * (id & 15)) = yv[i];
    }
    RAW_BAR();
    STAMP(1, 2);
    const int r2 = tz & 15, g2 = (tz >> 4) & 3;
    const int dl2 = 32 * dh + 16 * (g2 & 1) + 8 * (g2 >> 1);
#pragma unroll
    for (int bb = 0; bb < 2; ++bb) {
      float v[8];
#pragma unroll
      for (int i = 0; i < 4; ++i) {
        auto sw = __builtin_amdgcn_permlane16_swap(__float_as_uint(acc[0][bb][i]),
                                                   __float_as_uint(acc[1][bb][i]), false, false);
        v[i] = __uint_as_float(sw[0]);
        v[4 + i] = __uint_as_float(sw[1]);
      }
      const int tl = 32 * th + 16 * bb + r2;
      const u16* yp = ytile + dl2 * 72 + tl;
      const float inv_s = 1.0f / st[bb][1];
      float o[8];
#pragma unroll
      for (int e = 0; e < 8; ++e) {
        float xs = (e & 1) ? hi_f(xv[bb][e >> 1]) : lo_f(xv[bb][e >> 1]);
        float ds = (e & 1) ? hi_f(dv[e >> 1]) : lo_f(dv[e >> 1]);
        float bs = (e & 1) ? hi_f(bv[e >> 1]) : lo_f(bv[e >> 1]);
        float y = bf2f(yp[e * 72]);
        float xn = (xs - st[bb][0]) * inv_s;
        o[e] = (xs + (y + xn * ds)) + (v[e] + bs);
      }
      u32x4 ov = {pk_bf16(o[0], o[1]), pk_bf16(o[2], o[3]), pk_bf16(o[4], o[5]), pk_bf16(o[6], o[7])};
      *(u32x4*)(out + (size_t)(t0 + tl) * D + d0 + dl2) = ov;
    }
  }
  STAMP(1, 3);
}

__global__ __launch_bounds__(512, 4) void k2(const u16* __restrict__ Kr, const u16* __restrict__ xnT,
                                             u16* yT, u32* cnt, const u16* __restrict__ h,
                                             const u16* __restrict__ w2, const u16* __restrict__ b2,
                                             const u16* __restrict__ x, const u16* __restrict__ Dsk,
                                             const float* __restrict__ stats, u16* __restrict__ out) {
  __shared__ __attribute__((aligned(16))) char smem[K2_SMEM];
  if (blockIdx.x < 256) conv_role(smem, Kr, xnT, yT, cnt);
  if (blockIdx.x >= 256) gemm2_role(smem, h, w2, b2, x, Dsk, stats, yT, cnt, out);
}

extern "C" void kernel_launch(void* const* d_in, const int* in_sizes, int n_in, void* d_out,
                              int out_size, void* d_ws, size_t ws_size, hipStream_t stream) {
  const u16* x = (const u16*)d_in[0];
  const u16* logA = (const u16*)d_in[1];
  const u16* Aim = (const u16*)d_in[2];
  const u16* Cr = (const u16*)d_in[3];
  const u16* Ci = (const u16*)d_in[4];
  const u16* logdt = (const u16*)d_in[5];
  const u16* Dsk = (const u16*)d_in[6];
  const u16* w1 = (const u16*)d_in[8];
  const u16* b1 = (const u16*)d_in[9];
  const u16* w2 = (const u16*)d_in[10];
  const u16* b2 = (const u16*)d_in[11];
  char* ws = (char*)d_ws;
  u32* cnt = (u32*)ws; ws += 4096;
  float* stats = (float*)ws; ws += L * 2 * 4;
  u16* h = (u16*)ws; ws += (size_t)L * F * 2;
  u16* xnT = (u16*)ws; ws += (size_t)D * L * 2;
  u16* yT = (u16*)ws; ws += (size_t)D * L * 2;
  u16* Kr = (u16*)ws; ws += (size_t)D * KRP * 2;
  k1<<<256, 768, 0, stream>>>(x, w1, b1, w2, logA, Aim, Cr, Ci, logdt, h, xnT, Kr, stats, cnt);
  k2<<<512, 512, 0, stream>>>(Kr, xnT, yT, cnt, h, w2, b2, x, Dsk, stats, (u16*)d_out);
}
```

```cpp
#include <hip/hip_runtime.h>
#include <hip/hip_bf16.h>
#include <stdint.h>

typedef unsigned short u16;
typedef unsigned int u32;
typedef unsigned long long u64;
typedef __attribute__((ext_vector_type(8))) short bf16x8;
typedef __attribute__((ext_vector_type(4))) short s16x4;
typedef __attribute__((ext_vector_type(4))) float f32x4;
typedef __attribute__((ext_vector_type(4))) unsigned int u32x4;
typedef __attribute__((ext_vector_type(2))) unsigned int u32x2;
typedef __attribute__((ext_vector_type(2))) __bf16 bf2_t;
typedef __attribute__((ext_vector_type(2))) float f2_t;

#define DEV __device__ __forceinline__

constexpr int L = 2048, D = 512, NS = 128, F = 1792;
constexpr int KRP = 2064;

DEV float bf2f(u16 v) { return __uint_as_float(((u32)v) << 16); }
DEV u32 pk_bf16(float lo, float hi) {
  f2_t v = {lo, hi};
  bf2_t r = __builtin_convertvector(v, bf2_t);
  return __builtin_bit_cast(u32, r);
}
DEV float lo_f(u32 p) { return __uint_as_float(p << 16); }
DEV float hi_f(u32 p) { return __uint_as_float(p & 0xffff0000u); }
DEV float rbf(float f) { return lo_f(pk_bf16(f, f)); }

#define WAIT_V(n) asm volatile("s_waitcnt vmcnt(" #n ")" ::: "memory")
#define CFENCE() asm volatile("" ::: "memory")

DEV f32x4 mfma16(bf16x8 a, bf16x8 b, f32x4 c) {
  return __builtin_amdgcn_mfma_f32_16x16x32_bf16(a, b, c, 0, 0, 0);
}

typedef __attribute__((address_space(3))) s16x4 lds_s16x4;
DEV s16x4 tr_read(const char* p) {
  return __builtin_amdgcn_ds_read_tr16_b64_v4i16((lds_s16x4*)p);
}

constexpr int K1_RING = 131072;
constexpr int K1_KP = K1_RING;
constexpr int K1_CS = K1_KP + 24576;
constexpr int K1_PF = K1_CS + 512 + 1024;
constexpr int K1_SMEM = K1_PF + 4096;

#define GLDS16(src, dst)                                                                     \
  __builtin_amdgcn_global_load_lds((const __attribute__((address_space(1))) void*)(src),     \
                                   (__attribute__((address_space(3))) void*)(dst), 16, 0, 0)
#define LGKM0() __builtin_amdgcn_s_waitcnt(0xC07F)
#define RAW_BAR() do { LGKM0(); CFENCE(); __builtin_amdgcn_s_barrier(); CFENCE(); } while (0)

DEV int kpos_row(int rt, int row) {
  if (rt == 0) return row;
  if (rt == 1) return (row < 8) ? (256 + 2 * row) : (512 + 4 * (row & 3) + 256 * ((row >> 2) & 1));
  return (row < 8) ? (1024 + 8 * (row & 1) + 256 * (row >> 1)) : ((row == 8) ? 2048 : 0);
}
struct KIdxTab { u16 v[KRP]; };
constexpr int c_rne_bf16(int l) {
  if (l < 256) return l;
  int sh = (l < 512) ? 1 : (l < 1024) ? 2 : 3;
  return ((l + (1 << (sh - 1)) - 1 + ((l >> sh) & 1)) >> sh) << sh;
}
constexpr int c_kpos_index(int p) {
  int rt = 0, row = 0, lo = 0;
  if (p < 256) { rt = 0; row = p & 15; lo = p >> 4; }
  else if (p < 512) { int q = p - 256; rt = 1; row = (q & 15) >> 1; lo = q >> 4; }
  else if (p < 1024) { int q = p - 512; rt = 1; row = 8 + ((q & 15) >> 2) + 4 * (q >> 8); lo = (q & 255) >> 4; }
  else if (p < 2048) { int q = p - 1024; rt = 2; row = ((q & 15) >> 3) + 2 * (q >> 8); lo = (q & 255) >> 4; }
  else { rt = 2; row = 8; lo = 0; }
  return (rt * 64 + lo + 16 * (row >> 2)) * 4 + (row & 3);
}
constexpr KIdxTab make_kidx() {
  KIdxTab t{};
  for (int y = 0; y < KRP; ++y) {
    int l = 2047 - y;
    t.v[y] = (l < 0) ? (u16)0xFFFF : (u16)c_kpos_index(c_rne_bf16(l));
  }
  return t;
}
__device__ __attribute__((aligned(16))) const KIdxTab g_kidx = make_kidx();

typedef __attribute__((ext_vector_type(8))) _Float16 f16x8;
typedef __attribute__((ext_vector_type(2))) _Float16 h2_t;
DEV u32 pk_f16(float lo, float hi) {
  f2_t v = {lo, hi};
  h2_t r = __builtin_convertvector(v, h2_t);
  return __builtin_bit_cast(u32, r);
}
DEV f32x4 mfma16h(u32x4 a, u32x4 b, f32x4 c) {
  return __builtin_amdgcn_mfma_f32_16x16x32_f16(__builtin_bit_cast(f16x8, a), __builtin_bit_cast(f16x8, b), c, 0, 0, 0);
}
DEV bf16x8 tr_frag(const char* p, int d2) {
  s16x4 a0 = tr_read(p);
  s16x4 a1 = tr_read(p + d2);
  return (bf16x8){a0[0], a0[1], a0[2], a0[3], a1[0], a1[1], a1[2], a1[3]};
}

template <int NH>
DEV void gemm1_loop(char* smem, int Tl, int xadr0, const int (&adr)[4],
                    f32x4 (&acc)[2][4], f32x4& cs, f32x4 (&sm)[2], f32x4 (&sq)[2]) {
  const short one = 0x3f80;
  const bf16x8 ones = {one, one, one, one, one, one, one, one};
  constexpr int NT = NH ? 3 : 4;
  bf16x8 xf[2][2], a[2][NT];
  u32 msk[NT];
#pragma unroll
  for (int T = 0; T < NT; ++T) msk[T] = (Tl == T) ? 0xffffffffu : 0u;
  auto rd = [&](int u, int slot) {
    const int s = u >> 1, k2 = u & 1;
    const char* xb = smem + (s & 3) * 32768;
    const char* wb = xb + 16384 + k2 * 8192;
    xf[slot][0] = *(const bf16x8*)(xb + (xadr0 ^ (64 * k2)));
    xf[slot][1] = *(const bf16x8*)(xb + (xadr0 ^ (64 * k2)) + 2048);
#pragma unroll
    for (int T = 0; T < NT; ++T) a[slot][T] = tr_frag(wb + adr[T], 1024);
  };
  auto unit = [&](int u, bool more) {
    const int c = u & 1;
    if (more) {
      if (u & 1) RAW_BAR();
      rd((u + 1) & 7, c ^ 1);
    }
    if (NH) {
      sm[0] = mfma16(ones, xf[c][0], sm[0]);
      sq[0] = mfma16(xf[c][0], xf[c][0], sq[0]);
      sm[1] = mfma16(ones, xf[c][1], sm[1]);
      sq[1] = mfma16(xf[c][1], xf[c][1], sq[1]);
    }
    {
      u32x4 ac = __builtin_bit_cast(u32x4, a[c][0]) & msk[0];
#pragma unroll
      for (int T = 1; T < NT; ++T) ac |= __builtin_bit_cast(u32x4, a[c][T]) & msk[T];
      cs = mfma16(__builtin_bit_cast(bf16x8, ac), ones, cs);
    }
#pragma unroll
    for (int T = 0; T < NT; ++T) {
      acc[0][T] = mfma16(a[c][T], xf[c][0], acc[0][T]);
      acc[1][T] = mfma16(a[c][T], xf[c][1], acc[1][T]);
    }
    if (more) {
#pragma unroll
      for (int i = 0; i < 6; ++i) {
        __builtin_amdgcn_sched_group_barrier(0x8, 1, 0);
        __builtin_amdgcn_sched_group_barrier(0x100, 2, 0);
      }
    }
    __builtin_amdgcn_sched_barrier(0);
  };
  CFENCE(); __builtin_amdgcn_s_barrier(); CFENCE();
  rd(0, 0);
#pragma unroll
  for (int u = 0; u < 16; ++u) unit(u & 7, u + 1 < 16);
}

template <int NH>
DEV void gemm1_epi(const float* cs_lds, const float* st_lds, const u32x2 (&b1v)[4],
                   u16* __restrict__ h, int t0, int n0, int wt, int r, int g, f32x4 (&acc)[2][4]) {
  constexpr int NT = NH ? 3 : 4;
  constexpr int TB = NH ? 4 : 0;
  float mean_b[2], inv_s[2];
#pragma unroll
  for (int tt = 0; tt < 2; ++tt) {
    f2_t st = *(const f2_t*)(st_lds + 2 * (32 * wt + 16 * tt + r));
    mean_b[tt] = st[0];
    inv_s[tt] = 1.0f / st[1];
  }
  u32x2 hp[2][NT];
#pragma unroll
  for (int T = 0; T < NT; ++T) {
    f32x4 cs = *(const f32x4*)(cs_lds + 16 * (TB + T) + 4 * g);
    u32x2 bb = b1v[T];
    f32x4 bv = {lo_f(bb[0]), hi_f(bb[0]), lo_f(bb[1]), hi_f(bb[1])};
#pragma unroll
    for (int tt = 0; tt < 2; ++tt) {
      const float ms = mean_b[tt] * inv_s[tt];
      f32x4 v = acc[tt][T] * inv_s[tt] + (bv - cs * ms);
      const float c1 = -2.0f * 0.7978845608028654f * 1.4426950408889634f;
      const float c2 = c1 * 0.044715f;
      f32x4 z = v * (v * v * c2 + c1);
      f32x4 d;
#pragma unroll
      for (int i = 0; i < 4; ++i) d[i] = __builtin_amdgcn_exp2f(z[i]);
      d = d + 1.0f;
#pragma unroll
      for (int i = 0; i < 4; ++i) d[i] = __builtin_amdgcn_rcpf(d[i]);
      v = v * d;
      hp[tt][T][0] = pk_bf16(v[0], v[1]);
      hp[tt][T][1] = pk_bf16(v[2], v[3]);
    }
  }
#pragma unroll
  for (int tt = 0; tt < 2; ++tt) {
    u16* hrow = h + (size_t)(t0 + 32 * wt + 16 * tt + r) * F + n0 + 16 * TB;
#pragma unroll
    for (int T = 0; T + 1 < NT; T += 2) {
      auto s0 = __builtin_amdgcn_permlane16_swap(hp[tt][T][0], hp[tt][T + 1][0], false, false);
      auto s1 = __builtin_amdgcn_permlane16_swap(hp[tt][T][1], hp[tt][T + 1][1], false, false);
      u32x4 o = {s0[0], s1[0], s0[1], s1[1]};
      *(u32x4*)(hrow + 16 * (T + (g & 1)) + 8 * (g >> 1)) = o;
    }
    if (NH) *(u32x2*)(hrow + 32 + 4 * g) = hp[tt][2];
  }
}

template <int OP>
DEV void k1_loader(char* smem, const u16* __restrict__ x, const u16* __restrict__ w1, int t0, int n0, int l,
                   int hf, int bj, int bi1, u16* __restrict__ xnT, const u16* __restrict__ w2pf,
                   f32x4* __restrict__ Ktab) {
  const u16* src[2];
#pragma unroll
  for (int v = 0; v < 2; ++v) {
    if (OP == 0) {
      src[v] = x + (size_t)(t0 + 64 * hf + (l >> 3)) * D + 8 * ((l & 7) ^ ((l >> 4) + 4 * v));
    } else {
      int c16 = ((l & 15) - 2 * ((l >> 4) + 4 * v)) & 15;
      c16 = c16 < 14 ? c16 : c16 - 14;
      src[v] = w1 + (size_t)(32 * hf + (l >> 4)) * F + n0 + c16 * 8;
    }
  }
  char* dst0 = smem + OP * 16384 + hf * 8192;
  auto stage = [&](int s) {
    char* dst = dst0 + (s & 3) * 32768;
#pragma unroll
    for (int j = 0; j < 8; ++j) {
      if (OP == 0) GLDS16(src[j & 1] + (size_t)(8 * j) * D + 64 * s, dst + j * 1024);
      else GLDS16(src[(j >> 1) & 1] + (size_t)(64 * s + 4 * j) * F, dst + j * 1024);
    }
  };
  stage(0); stage(1); stage(2); stage(3);
  {
    const int lw_ = 2 * OP + hf;
    const int c14 = (l & 15) < 14 ? (l & 15) : 0;
    const u16* wp = w1 + (size_t)(256 + 128 * bi1 + 32 * lw_ + (l >> 4)) * F + n0 + 8 * c14;
#pragma unroll
    for (int j = 0; j < 8; ++j) GLDS16(wp + (size_t)(4 * j) * F, smem + K1_PF + (j & 3) * 1024);
    GLDS16(x + (size_t)(t0 + 8 * bj + 2 * lw_ + (l >> 5)) * D + 256 + 8 * (l & 31), smem + K1_PF);
  }
  GLDS16(w2pf + 8 * l, smem + K1_PF);
  GLDS16(w2pf + 384 + 8 * l, smem + K1_PF + 1024);
  WAIT_V(35);
  __builtin_amdgcn_s_barrier(); CFENCE();
  {
    const f32x4* Kp = (const f32x4*)(smem + K1_KP);
    const int id0 = 96 * (2 * OP + hf) + l;
#pragma unroll
    for (int i = 0; i < 2; ++i) {
      const int id = id0 + 64 * i;
      if (l + 64 * i < 96) {
        const int c3 = id / 192, e = id % 192;
        f32x4 v = (Kp[(4 * c3) * 192 + e] + Kp[(4 * c3 + 1) * 192 + e]) +
                  (Kp[(4 * c3 + 2) * 192 + e] + Kp[(4 * c3 + 3) * 192 + e]);
        Ktab[id] = v;
      }
    }
    LGKM0();
  }
#pragma unroll
  for (int s = 1; s < 8; ++s) {
    if (s <= 3) WAIT_V(29);
    else if (s <= 5) WAIT_V(16);
    else if (s == 6) WAIT_V(8);
    else WAIT_V(0);
    __builtin_amdgcn_s_barrier(); CFENCE();
    if (s + 3 < 8) stage(s + 3);
  }
  const int lw = 2 * OP + hf;
  u32x4 xs[2];
#pragma unroll
  for (int i = 0; i < 2; ++i)
    xs[i] = *(const u32x4*)(x + (size_t)(t0 + 32 * lw + 16 * i + (l >> 2)) * D + 32 * bj + 8 * (l & 3));
  CFENCE(); __builtin_amdgcn_s_barrier(); CFENCE();
  const float* st_lds = (const float*)(smem + K1_CS + 512);
  u16* tb = (u16*)(smem + K1_KP + lw * 4096);
#pragma unroll
  for (int i = 0; i < 2; ++i) {
    const int tl = 16 * i + (l >> 2);
    f2_t st = *(const f2_t*)(st_lds + 2 * (32 * lw + tl));
    const float is = 1.0f / st[1];
#pragma unroll
    for (int e = 0; e < 4; ++e) {
      float v0 = rbf(lo_f(xs[i][e]) - st[0]) * is;
      float v1 = rbf(hi_f(xs[i][e]) - st[0]) * is;
      u32 pk = pk_bf16(v0, v1);
      tb[(8 * (l & 3) + 2 * e) * 40 + tl] = (u16)(pk & 0xffffu);
      tb[(8 * (l & 3) + 2 * e + 1) * 40 + tl] = (u16)(pk >> 16);
    }
  }
  LGKM0();
  CFENCE();
#pragma unroll
  for (int i = 0; i < 2; ++i) {
    const int dd = 16 * i + (l >> 2), ck = l & 3;
    u32x4 o = *(const u32x4*)(tb + dd * 40 + 8 * ck);
    *(u32x4*)(xnT + (size_t)(32 * bj + dd) * L + t0 + 32 * lw + 8 * ck) = o;
  }
}

__global__ __launch_bounds__(768, 3) void k1(const u16* __restrict__ x, const u16* __restrict__ w1,
                                             const u16* __restrict__ b1, const u16* __restrict__ w2,
                                             const u16* __restrict__ logA, const u16* __restrict__ Aim,
                                             const u16* __restrict__ Cr, const u16* __restrict__ Ci,
                                             const u16* __restrict__ logdt, u16* __restrict__ h,
                                             u16* __restrict__ xnT, u16* __restrict__ Kr,
                                             float* __restrict__ stats, u32* __restrict__ cnt) {
  __shared__ __attribute__((aligned(16))) char smem[K1_SMEM];
  const int b = blockIdx.x;
  const int xcd = b & 7, idx = b >> 3;
  const int bi = 2 * xcd + (idx >> 4), bj = idx & 15;
  const int t0 = bi * 128, n0 = bj * 112;
  const int tid = threadIdx.x, l = tid & 63, r = l & 15, g = l >> 4;
  const int w = __builtin_amdgcn_readfirstlane(tid >> 6);
  if (w >= 10) { k1_loader<1>(smem, x, w1, t0, n0, l, w & 1, bj, bi & 1, xnT, w2 + (size_t)b * 3584 + (2 + (w & 1)) * 896, (f32x4*)Kr + (size_t)b * 384); return; }
  if (w >= 8) { k1_loader<0>(smem, x, w1, t0, n0, l, w & 1, bj, bi & 1, xnT, w2 + (size_t)b * 3584 + (w & 1) * 896, (f32x4*)Kr + (size_t)b * 384); return; }
  const int wt = w & 3, nh = w >> 2;
  if (b == 0 && tid < 8) cnt[32 * tid] = 0u;

  u32x2 b1v[4];
#pragma unroll
  for (int T = 0; T < 4; ++T) b1v[T] = *(const u32x2*)(b1 + n0 + 16 * (nh ? (T < 3 ? 4 + T : 6) : T) + 4 * g);
  const int cc = w >> 2, ch = 2 * b + cc, qn = w & 3;
  f32x4* Kp = (f32x4*)(smem + K1_KP);
  {
    const int base = ch * NS + 32 * qn + 8 * g;
    bf16x8 vla = *(const bf16x8*)(logA + base);
    bf16x8 vai = *(const bf16x8*)(Aim + base);
    bf16x8 vcr = *(const bf16x8*)(Cr + base);
    bf16x8 vci = *(const bf16x8*)(Ci + base);
    const u32 ldt2 = ((const u32*)logdt)[b];
    const float dt = rbf(expf(cc ? hi_f(ldt2) : lo_f(ldt2)));
    const float pF = (float)(16 * r);
    float a2[8], br[8], cr[8], ci[8];
#pragma unroll
    for (int j = 0; j < 8; ++j) {
      float a = -rbf(__expf(bf2f((u16)vla[j]))) * dt;
      a2[j] = a * 1.4426950408889634f;
      br[j] = (bf2f((u16)vai[j]) * dt) * 0.15915494309189535f;
      cr[j] = bf2f((u16)vcr[j]) * dt;
      ci[j] = bf2f((u16)vci[j]) * dt;
    }
    float mx = 0.f;
#pragma unroll
    for (int j = 0; j < 8; ++j) mx = fmaxf(mx, fabsf(cr[j]) + fabsf(ci[j]));
    mx = fmaxf(mx, __shfl_xor(mx, 16));
    mx = fmaxf(mx, __shfl_xor(mx, 32));
    u32 ex_ = (__float_as_uint(mx) >> 23) & 255u;
    ex_ = ex_ < 1u ? 1u : (ex_ > 253u ? 253u : ex_);
    const float S = __uint_as_float((254u - ex_) << 23);
    const float outS = 2.0f * __uint_as_float(ex_ << 23);
    u32x4 Fr, Fi;
#pragma unroll
    for (int j2 = 0; j2 < 4; ++j2) {
      float fr[2], fi[2];
#pragma unroll
      for (int e = 0; e < 2; ++e) {
        int j = 2 * j2 + e;
        float ex = __builtin_amdgcn_exp2f(a2[j] * pF) * S;
        float an = __builtin_amdgcn_fractf(br[j] * pF);
        float er = ex * __builtin_amdgcn_cosf(an);
        float ei = ex * __builtin_amdgcn_sinf(an);
        fr[e] = cr[j] * er - ci[j] * ei;
        fi[e] = -(cr[j] * ei + ci[j] * er);
      }
      Fr[j2] = pk_f16(fr[0], fr[1]);
      Fi[j2] = pk_f16(fi[0], fi[1]);
    }
#pragma unroll
    for (int rt = 0; rt < 3; ++rt) {
      const float pG = (float)kpos_row(rt, r);
      u32x4 Gr, Gi;
#pragma unroll
      for (int j2 = 0; j2 < 4; ++j2) {
        float gr[2], gi[2];
#pragma unroll
        for (int e = 0; e < 2; ++e) {
          int j = 2 * j2 + e;
          float ex = __builtin_amdgcn_exp2f(a2[j] * pG);
          float an = __builtin_amdgcn_fractf(br[j] * pG);
          gr[e] = ex * __builtin_amdgcn_cosf(an);
          gi[e] = ex * __builtin_amdgcn_sinf(an);
        }
        Gr[j2] = pk_f16(gr[0], gr[1]);
        Gi[j2] = pk_f16(gi[0], gi[1]);
      }
      f32x4 a = {0.f, 0.f, 0.f, 0.f};
      a = mfma16h(Gr, Fr, a);
      a = mfma16h(Gi, Fi, a);
      Kp[(w * 3 + rt) * 64 + l] = a * outS;
    }
  }
  LGKM0();

  const int q = r >> 2, p = r & 3;
  const int rowoff = (8 * g + q) * 256 + 8 * (p & 1);
  const int prot = (p >> 1) + 2 * q + 8 * (g & 1);
  int adr[4];
#pragma unroll
  for (int T = 0; T < 4; ++T) adr[T] = rowoff + (((2 * (4 * nh + T) + prot) & 15) << 4);
  const int Tc = nh ? (4 + (wt > 2 ? 2 : wt)) : wt;
  const int xadr0 = (32 * wt + r) * 128 + ((g ^ ((r >> 1) & 7)) << 4);

  f32x4 acc[2][4];
#pragma unroll
  for (int tt = 0; tt < 2; ++tt)
#pragma unroll
    for (int T = 0; T < 4; ++T) acc[tt][T] = (f32x4){0.f, 0.f, 0.f, 0.f};
  f32x4 cs = {0.f, 0.f, 0.f, 0.f};
  f32x4 sm[2] = {{0.f, 0.f, 0.f, 0.f}, {0.f, 0.f, 0.f, 0.f}}, sq[2] = {{0.f, 0.f, 0.f, 0.f}, {0.f, 0.f, 0.f, 0.f}};
  float* cs_lds = (float*)(smem + K1_CS);
  float* st_lds = (float*)(smem + K1_CS + 512);
  if (nh == 0) {
    gemm1_loop<0>(smem, Tc, xadr0, adr, acc, cs, sm, sq);
    if (r == 0) *(f32x4*)(cs_lds + 16 * Tc + 4 * g) = cs;
    RAW_BAR();
    gemm1_epi<0>(cs_lds, st_lds, b1v, h, t0, n0, wt, r, g, acc);
  } else {
    gemm1_loop<1>(smem, Tc - 4, xadr0, adr, acc, cs, sm, sq);
    if (r == 0) *(f32x4*)(cs_lds + 16 * Tc + 4 * g) = cs;
#pragma unroll
    for (int tt = 0; tt < 2; ++tt) {
      float cand = (p == 0) ? sq[tt][0] : (p == 1) ? sq[tt][1] : (p == 2) ? sq[tt][2] : sq[tt][3];
      float sumsq = __shfl(cand, r + 16 * q);
      float mean = sm[tt][0] * (1.0f / D);
      float var = fmaxf(sumsq * (1.0f / D) - mean * mean, 0.f);
      float m = rbf(mean), v = rbf(var);
      float s = rbf(sqrtf(rbf(v + 1e-5f)));
      if (g == 0) {
        f2_t st = {m, s};
        *(f2_t*)(st_lds + 2 * (32 * wt + 16 * tt + r)) = st;
        if (bj == 0) *(f2_t*)(stats + 2 * (t0 + 32 * wt + 16 * tt + r)) = st;
      }
    }
    RAW_BAR();
    gemm1_epi<1>(cs_lds, st_lds, b1v, h, t0, n0, wt, r, g, acc);
  }
}

constexpr int CPITCH = 4160;
constexpr int CH_BYTES = 8 * CPITCH + 4608;
constexpr int K2_SMEM = 81920;

DEV u32 funnel16(u32 hi, u32 lo) { return __builtin_amdgcn_alignbyte(hi, lo, 2); }

DEV void conv_role(char* smem, const u16* __restrict__ Kr, const u16* __restrict__ xnT,
                   u16* __restrict__ yT, u32* __restrict__ cnt) {
  const int c = blockIdx.x;
  const int tid = threadIdx.x, l = tid & 63, r = l & 15, g = l >> 4;
  const int w = __builtin_amdgcn_readfirstlane(tid >> 6);
  {
    const int c2 = tid >> 8, ck = tid & 255;
    float* Kt = (float*)(smem + 2 * CH_BYTES);
    f32x4 kv;
    if (tid < 384) kv = ((const f32x4*)Kr)[(size_t)c * 384 + tid];
    u32x4 uu = *(const u32x4*)(xnT + (size_t)(2 * c) * L + 8 * tid);
    const u32x4 ia = *(const u32x4*)(g_kidx.v + 8 * ck);
    const u32x4 ib = *(const u32x4*)(g_kidx.v + 8 * ck + 8);
    if (tid < 384) ((f32x4*)Kt)[tid] = kv;
    *(u32x4*)(smem + c2 * CH_BYTES + 8 * CPITCH + 512 + 16 * ck) = uu;
    if (ck < 32) *(u32x4*)(smem + c2 * CH_BYTES + 8 * CPITCH + 16 * ck) = (u32x4){0u, 0u, 0u, 0u};
    if (ck == 255) {
#pragma unroll
      for (int s = 0; s < 8; ++s)
        *(u32x4*)(smem + c2 * CH_BYTES + 16 * 256 + s * CPITCH) = (u32x4){0u, 0u, 0u, 0u};
    }
    __syncthreads();
    const float* kt = Kt + c2 * 768;
    char* base = smem + c2 * CH_BYTES + 16 * ck;
    u32 d[8];
#pragma unroll
    for (int e2 = 0; e2 < 8; ++e2) {
      u32 pr = e2 < 4 ? ia[e2 & 3] : ib[e2 & 3];
      u32 i0 = pr & 0xffffu, i1 = pr >> 16;
      float v0 = kt[i0 == 0xffffu ? 0u : i0], v1 = kt[i1 == 0xffffu ? 0u : i1];
      d[e2] = pk_bf16(i0 == 0xffffu ? 0.f : v0, i1 == 0xffffu ? 0.f : v1);
    }
#pragma unroll
    for (int s = 0; s < 8; ++s) {
      const int e = 7 - s;
      u32x4 o;
#pragma unroll
      for (int i = 0; i < 4; ++i)
        o[i] = (e & 1) ? funnel16(d[(e + 1) / 2 + i], d[(e - 1) / 2 + i]) : d[e / 2 + i];
      *(u32x4*)(base + s * CPITCH) = o;
    }
  }
  __syncthreads();
  const int cc = w >> 2, ch = 2 * c + cc, wq = w & 3;
  f32x4 acc[8];
#pragma unroll
  for (int i = 0; i < 8; ++i) acc[i] = (f32x4){0.f, 0.f, 0.f, 0.f};
  {
    const char* base = smem + cc * CH_BYTES;
    const char* ab = base + (r & 7) * CPITCH + 2 * (2048 - 8 * g - 8 * (r >> 3)) - 64 * (2 * wq);
    const char* bb = base + 8 * CPITCH + 2 * (256 + 16 * r - 8 * g + 8) - 64 * (2 * wq);
#pragma unroll
    for (int cl = 0; cl < 2; ++cl) {
      bf16x8 A[8], B[8];
#pragma unroll
      for (int pq = 0; pq < 8; ++pq) {
        A[pq] = *(const bf16x8*)(ab - 64 * cl - 512 * pq);
        B[pq] = *(const bf16x8*)(bb - 64 * cl + 512 * pq);
      }
#pragma unroll
      for (int p = 0; p < 8; ++p)
#pragma unroll
        for (int i = p; i < 8; ++i) acc[i] = mfma16(A[p], B[i - p], acc[i]);
    }
  }
  __syncthreads();
  f32x4* xb = (f32x4*)(smem + cc * CH_BYTES);
#pragma unroll
  for (int i = 0; i < 8; ++i) xb[(wq * 8 + i) * 64 + l] = acc[i];
  __syncthreads();
  {
    u16* yrow = yT + (size_t)ch * L + 16 * r + 4 * g + 512 * wq;
#pragma unroll
    for (int i = 0; i < 2; ++i) {
      const f32x4* p = xb + (2 * wq + i) * 64 + l;
      f32x4 v = (p[0] + p[512]) + (p[1024] + p[1536]);
      u64 o = (u64)pk_bf16(v[0], v[1]) | ((u64)pk_bf16(v[2], v[3]) << 32);
      __hip_atomic_store((u64*)(yrow + 256 * i), o, __ATOMIC_RELAXED, __HIP_MEMORY_SCOPE_AGENT);
    }
  }
  WAIT_V(0);
  __syncthreads();
  if (tid == 0)
    __hip_atomic_fetch_add(cnt + 32 * (c >> 5), 1u, __ATOMIC_RELAXED, __HIP_MEMORY_SCOPE_AGENT);
}

constexpr int G2_NR = 5, G2_NST = 28;

template <int WHICH>
DEV void g2_loader(char* smem, const u16* __restrict__ h, const u16* __restrict__ w2, int t0, int d0, int l,
                   int hf) {
  const u16* src[2];
#pragma unroll
  for (int v = 0; v < 2; ++v) {
    if (WHICH == 0) src[v] = h + (size_t)(t0 + 32 * hf + (l >> 3)) * F + 8 * ((l & 7) ^ ((l >> 4) + 4 * v));
    else src[v] = w2 + (size_t)(32 * hf + (l >> 3)) * D + d0 + 8 * ((l & 7) ^ ((((l >> 4) & 1) | (v << 1)) << 1));
  }
  char* dst0 = smem + WHICH * 8192 + hf * 4096;
  auto stage = [&](int s) {
    char* dst = dst0 + (s % G2_NR) * 16384;
#pragma unroll
    for (int i = 0; i < 4; ++i) {
      if (WHICH == 0) GLDS16(src[i & 1] + (size_t)(8 * i) * F + 64 * s, dst + i * 1024);
      else GLDS16(src[i & 1] + (size_t)(64 * s + 8 * i) * D, dst + i * 1024);
    }
  };
#pragma unroll
  for (int s = 0; s < G2_NR; ++s) stage(s);
  WAIT_V(16);
  __builtin_amdgcn_s_barrier(); CFENCE();
#pragma unroll
  for (int s = 0; s <= G2_NST - 2; ++s) {
    if (s + 4 <= G2_NST - 1) WAIT_V(12);
    else if (s + 3 == G2_NST - 1) WAIT_V(8);
    else if (s + 2 == G2_NST - 1) WAIT_V(4);
    else WAIT_V(0);
    __builtin_amdgcn_s_barrier(); CFENCE();
    if (s + G2_NR < G2_NST) stage(s + G2_NR);
  }
}

DEV void gemm2_role(char* smem, const u16* __restrict__ h, const u16* __restrict__ w2,
                    const u16* __restrict__ b2, const u16* __restrict__ x,
                    const u16* __restrict__ Dsk, const float* __restrict__ stats,
                    const u16* yT, u32* cnt, u16* __restrict__ out) {
  const int b = blockIdx.x - 256;
  const int xcd = b & 7, idx = b >> 3;
  const int ti = 4 * xcd + (idx >> 3), dj = idx & 7;
  const int t0 = 64 * ti, d0 = 64 * dj;
  const int tid = threadIdx.x, l = tid & 63, r = l & 15, g = l >> 4;
  const int w = __builtin_amdgcn_readfirstlane(tid >> 6);
  if (w >= 6) { g2_loader<1>(smem, h, w2, t0, d0, l, w & 1); return; }
  if (w >= 4) { g2_loader<0>(smem, h, w2, t0, d0, l, w & 1); return; }
  const int dh = w >> 1, th = w & 1;
  constexpr int NR = G2_NR, NST = G2_NST;

  const int q = r >> 2, p = r & 3;
  const int fr = ((q >> 1) & 1) | ((g & 1) << 1);
  int aadr[2];
#pragma unroll
  for (int a = 0; a < 2; ++a)
    aadr[a] = 8192 + (8 * g + q) * 128 + (((2 * (2 * dh + a) + (p >> 1)) ^ (fr << 1)) << 4) + 8 * (p & 1);
  const int badr = (32 * th + r) * 128 + ((g ^ ((r >> 1) & 7)) << 4);

  u32x4 xv[2], dv, bv;
  f2_t st[2];
  u64 yv[4];

  f32x4 acc[2][2];
#pragma unroll
  for (int i = 0; i < 2; ++i)
#pragma unroll
    for (int j = 0; j < 2; ++j) acc[i][j] = (f32x4){0.f, 0.f, 0.f, 0.f};

  bf16x8 af[2][2][2], bf[2][2][2];
  auto rd = [&](int s, int slot) {
    const char* sb = smem + (s % NR) * 16384;
#pragma unroll
    for (int k2 = 0; k2 < 2; ++k2) {
      bf[slot][k2][0] = *(const bf16x8*)(sb + (badr ^ (64 * k2)));
      bf[slot][k2][1] = *(const bf16x8*)(sb + (badr ^ (64 * k2)) + 2048);
#pragma unroll
      for (int a = 0; a < 2; ++a) af[slot][k2][a] = tr_frag(sb + aadr[a] + k2 * 4096, 512);
    }
  };
  u16* ytile = (u16*)(smem + ((NST - 5) % NR) * 16384);
  u32 cv = 0;
  bool got = false;
  auto step = [&](int sm10, bool more) {
    const int c = sm10 & 1;
    if (more) {
      RAW_BAR();
      rd(sm10 + 1, c ^ 1);
    }
#pragma unroll
    for (int k2 = 0; k2 < 2; ++k2)
#pragma unroll
      for (int a = 0; a < 2; ++a) {
        acc[a][0] = mfma16(af[c][k2][a], bf[c][k2][0], acc[a][0]);
        acc[a][1] = mfma16(af[c][k2][a], bf[c][k2][1], acc[a][1]);
      }
    if (more) {
#pragma unroll
      for (int i = 0; i < 6; ++i) {
        __builtin_amdgcn_sched_group_barrier(0x8, 1, 0);
        __builtin_amdgcn_sched_group_barrier(0x100, 2, 0);
      }
    }
    __builtin_amdgcn_sched_barrier(0);
  };
  CFENCE(); __builtin_amdgcn_s_barrier(); CFENCE();
  rd(0, 0);
#pragma unroll 1
  for (int s0 = 0; s0 < 20; s0 += 10) {
#pragma unroll
    for (int j = 0; j < 10; ++j) step(j, true);
  }
#pragma unroll
  for (int s = 20; s < NST; ++s) {
    step(s % 10, s + 1 < NST);
    if (s == 20) {
      cv = __hip_atomic_load(cnt + 32 * dj, __ATOMIC_RELAXED, __HIP_MEMORY_SCOPE_AGENT);
      __builtin_amdgcn_sched_barrier(0);
    }
    if (s == 23) {
      got = __builtin_amdgcn_readfirstlane(cv) >= 32u;
      CFENCE();
      int tz = tid;
      asm volatile("" : "+v"(tz));
      if (got) {
#pragma unroll
        for (int i = 0; i < 4; ++i) {
          int id = tz + 256 * i;
          yv[i] = __hip_atomic_load((const u64*)(yT + (size_t)(d0 + (id >> 4)) * L + t0 + 4 * (id & 15)),
                                    __ATOMIC_RELAXED, __HIP_MEMORY_SCOPE_AGENT);
        }
      }
      const int r2 = tz & 15, g2 = (tz >> 4) & 3;
      const int dl2 = 32 * dh + 16 * (g2 & 1) + 8 * (g2 >> 1);
#pragma unroll
      for (int bb = 0; bb < 2; ++bb) {
        const int t = t0 + 32 * th + 16 * bb + r2;
        xv[bb] = *(const u32x4*)(x + (size_t)t * D + d0 + dl2);
        st[bb] = *(const f2_t*)(stats + 2 * t);
      }
      dv = *(const u32x4*)(Dsk + d0 + dl2);
      bv = *(const u32x4*)(b2 + d0 + dl2);
      __builtin_amdgcn_sched_barrier(0);
    }
  }
  {
    int tz = tid;
    asm volatile("" : "+v"(tz));
    if (!got) {
      u32 spins = 0;
      while (__hip_atomic_load(cnt + 32 * dj, __ATOMIC_RELAXED, __HIP_MEMORY_SCOPE_AGENT) < 32u) {
        __builtin_amdgcn_s_sleep(2);
        if (++spins > (1u << 24)) break;
      }
      CFENCE();
#pragma unroll
      for (int i = 0; i < 4; ++i) {
        int id = tz + 256 * i;
        yv[i] = __hip_atomic_load((const u64*)(yT + (size_t)(d0 + (id >> 4)) * L + t0 + 4 * (id & 15)),
                                  __ATOMIC_RELAXED, __HIP_MEMORY_SCOPE_AGENT);
      }
    }
#pragma unroll
    for (int i = 0; i < 4; ++i) {
      int id = tz + 256 * i;
      *(u64*)(ytile + (id >> 4) * 72 + 4 * (id & 15)) = yv[i];
    }
    RAW_BAR();
    const int r2 = tz & 15, g2 = (tz >> 4) & 3;
    const int dl2 = 32 * dh + 16 * (g2 & 1) + 8 * (g2 >> 1);
#pragma unroll
    for (int bb = 0; bb < 2; ++bb) {
      float v[8];
#pragma unroll
      for (int i = 0; i < 4; ++i) {
        auto sw = __builtin_amdgcn_permlane16_swap(__float_as_uint(acc[0][bb][i]),
                                                   __float_as_uint(acc[1][bb][i]), false, false);
        v[i] = __uint_as_float(sw[0]);
        v[4 + i] = __uint_as_float(sw[1]);
      }
      const int tl = 32 * th + 16 * bb + r2;
      const u16* yp = ytile + dl2 * 72 + tl;
      const float inv_s = 1.0f / st[bb][1];
      float o[8];
#pragma unroll
      for (int e = 0; e < 8; ++e) {
        float xs = (e & 1) ? hi_f(xv[bb][e >> 1]) : lo_f(xv[bb][e >> 1]);
        float ds = (e & 1) ? hi_f(dv[e >> 1]) : lo_f(dv[e >> 1]);
        float bs = (e & 1) ? hi_f(bv[e >> 1]) : lo_f(bv[e >> 1]);
        float y = bf2f(yp[e * 72]);
        float xn = (xs - st[bb][0]) * inv_s;
        o[e] = (xs + (y + xn * ds)) + (v[e] + bs);
      }
      u32x4 ov = {pk_bf16(o[0], o[1]), pk_bf16(o[2], o[3]), pk_bf16(o[4], o[5]), pk_bf16(o[6], o[7])};
      *(u32x4*)(out + (size_t)(t0 + tl) * D + d0 + dl2) = ov;
    }
  }
}

__global__ __launch_bounds__(512, 4) void k2(const u16* __restrict__ Kr, const u16* __restrict__ xnT,
                                             u16* yT, u32* cnt, const u16* __restrict__ h,
                                             const u16* __restrict__ w2, const u16* __restrict__ b2,
                                             const u16* __restrict__ x, const u16* __restrict__ Dsk,
                                             const float* __restrict__ stats, u16* __restrict__ out) {
  __shared__ __attribute__((aligned(16))) char smem[K2_SMEM];
  if (blockIdx.x < 256) conv_role(smem, Kr, xnT, yT, cnt);
  else gemm2_role(smem, h, w2, b2, x, Dsk, stats, yT, cnt, out);
}

extern "C" void kernel_launch(void* const* d_in, const int* in_sizes, int n_in, void* d_out,
                              int out_size, void* d_ws, size_t ws_size, hipStream_t stream) {
  const u16* x = (const u16*)d_in[0];
  const u16* logA = (const u16*)d_in[1];
  const u16* Aim = (const u16*)d_in[2];
  const u16* Cr = (const u16*)d_in[3];
  const u16* Ci = (const u16*)d_in[4];
  const u16* logdt = (const u16*)d_in[5];
  const u16* Dsk = (const u16*)d_in[6];
  const u16* w1 = (const u16*)d_in[8];
  const u16* b1 = (const u16*)d_in[9];
  const u16* w2 = (const u16*)d_in[10];
  const u16* b2 = (const u16*)d_in[11];
  char* ws = (char*)d_ws;
  u32* cnt = (u32*)ws; ws += 4096;
  float* stats = (float*)ws; ws += L * 2 * 4;
  u16* h = (u16*)ws; ws += (size_t)L * F * 2;
  u16* xnT = (u16*)ws; ws += (size_t)D * L * 2;
  u16* yT = (u16*)ws; ws += (size_t)D * L * 2;
  u16* Kr = (u16*)ws; ws += (size_t)D * KRP * 2;
  k1<<<256, 768, 0, stream>>>(x, w1, b1, w2, logA, Aim, Cr, Ci, logdt, h, xnT, Kr, stats, cnt);
  k2<<<512, 512, 0, stream>>>(Kr, xnT, yT, cnt, h, w2, b2, x, Dsk, stats, (u16*)d_out);
}
```
